```python
import torch

def _softmax(x, axis=-1):
    return torch.softmax(x, dim=axis)
def _gelu(x):
    return torch.nn.functional.gelu(x, approximate="tanh")
def _silu(x):
    return torch.nn.functional.silu(x)
def _sigmoid(x):
    return torch.sigmoid(x)
def _layernorm(x, axis=-1, eps=1e-5):
    m = x.mean(dim=axis, keepdim=True)
    v = x.var(dim=axis, keepdim=True, unbiased=False)
    return (x - m) / torch.sqrt(v + eps)
def _scatter_add(tgt, idx, src, axis=0):
    return tgt.clone().index_add_(axis, idx, src)
def _segment_sum(src, seg, n):
    out = torch.zeros((n,) + src.shape[1:], dtype=src.dtype)
    return out.index_add_(0, seg, src)
def _pad(x, pw):
    flat = tuple(v for pair in reversed(pw) for v in pair)
    return torch.nn.functional.pad(x, flat)

kernel_name = 'sequential_preLN__softmax_full_mha__dense_activation_gelu__prompt'

def setup_inputs():

    _w = (lambda a: (lambda t: t.to(torch.bfloat16) if t.is_floating_point() else t)(torch.as_tensor(a)))
    def _A_setup():
        B, S, D = 1, 2048, 640
        return {
            "x": _w((lambda *s: torch.randn(*s, dtype=torch.bfloat16) * 0.05)(B, S, D)),
            "wqkv": _w((lambda *s: torch.randn(*s, dtype=torch.bfloat16) * 0.05)(D, 3 * D)),
            "wo": _w((lambda *s: torch.randn(*s, dtype=torch.bfloat16) * 0.05)(D, D)),
        }
    _A_in = _A_setup()

    def _B_setup():
        B, S, D, F = 1, 2048, 640, 1728
        return {
            "x": _w((lambda *s: torch.randn(*s, dtype=torch.bfloat16) * 0.05)(B, S, D)),
            "w1": _w((lambda *s: torch.randn(*s, dtype=torch.bfloat16) * 1.6)(D, F) * (D ** -0.5)),
            "b1": _w((lambda *s: torch.randn(*s, dtype=torch.bfloat16) * 1.6)(F) * 0.01),
            "w2": _w((lambda *s: torch.randn(*s, dtype=torch.bfloat16) * 0.2)(F, D) * (F ** -0.5)),
            "b2": _w((lambda *s: torch.randn(*s, dtype=torch.bfloat16) * 0.2)(D) * 0.01),
        }
    _B_in = _B_setup()

    return {
        "A_x": _A_in["x"],
        "A_wqkv": _A_in["wqkv"],
        "A_wo": _A_in["wo"],
        "B_x": _B_in["x"],
        "B_w1": _B_in["w1"],
        "B_b1": _B_in["b1"],
        "B_w2": _B_in["w2"],
        "B_b2": _B_in["b2"],
    }

def reference(A_x, A_wqkv, A_wo, B_x, B_w1, B_b1, B_w2, B_b2):
    def _A_ref(x, wqkv, wo):
        B, S, D = x.shape
        H = 32; Dh = D // H
        qkv = (lambda x, *d: x.permute(*d))((x @ wqkv).reshape(B, S, 3, H, Dh), 2, 0, 3, 1, 4)
        q, k, v = qkv[0], qkv[1], qkv[2]
        scale = Dh ** -0.5
        scores = torch.einsum("bhqd,bhkd->bhqk", q, k) * scale
        p = _softmax(scores, axis=-1)
        o = torch.einsum("bhqk,bhkd->bhqd", p, v)
        return (lambda x, *d: x.permute(*d))(o, 0, 2, 1, 3).reshape(B, S, D) @ wo

    def _B_ref(x, w1, b1, w2, b2):
        h = torch.einsum("bsd,df->bsf", x, w1) + b1
        h = _gelu(h)
        return torch.einsum("bsf,fd->bsd", h, w2) + b2

    x = A_x
    h = x + _A_ref(_layernorm(x, axis=-1), A_wqkv, A_wo)
    h2 = h + _B_ref(_layernorm(h, axis=-1), B_w1, B_b1, B_w2, B_b2)
    return h2
```

```cpp
#include <hip/hip_runtime.h>
#include <hip/hip_bf16.h>
typedef unsigned short u16;
constexpr int S = 2048, D = 640, H = 32, DH = 20, F = 1728;
__device__ __forceinline__ float b2f(u16 v) { return __uint_as_float((unsigned)v << 16); }
__device__ __forceinline__ u16 f2b(float f) { unsigned u = __float_as_uint(f); u += 0x7fff + ((u >> 16) & 1); return (u16)(u >> 16); }

__global__ void k_ln(const u16* x, u16* y) {
  int r = blockIdx.x; __shared__ float red[2][4];
  float s = 0, q = 0;
  for (int c = threadIdx.x; c < D; c += 256) { float v = b2f(x[r * D + c]); s += v; }
  for (int o = 32; o; o >>= 1) s += __shfl_xor(s, o);
  if ((threadIdx.x & 63) == 0) red[0][threadIdx.x >> 6] = s;
  __syncthreads();
  float m = (red[0][0] + red[0][1] + red[0][2] + red[0][3]) / D;
  for (int c = threadIdx.x; c < D; c += 256) { float v = b2f(x[r * D + c]) - m; q += v * v; }
  for (int o = 32; o; o >>= 1) q += __shfl_xor(q, o);
  if ((threadIdx.x & 63) == 0) red[1][threadIdx.x >> 6] = q;
  __syncthreads();
  float rs = rsqrtf((red[1][0] + red[1][1] + red[1][2] + red[1][3]) / D + 1e-5f);
  for (int c = threadIdx.x; c < D; c += 256) y[r * D + c] = f2b((b2f(x[r * D + c]) - m) * rs);
}
template <int MODE> __global__ void k_gemm(const u16* A, const u16* B, u16* C, const u16* bias, const u16* res, int N, int K) {
  __shared__ float As[16][17], Bs[16][17];
  int tx = threadIdx.x & 15, ty = threadIdx.x >> 4, row = blockIdx.y * 16 + ty, col = blockIdx.x * 16 + tx;
  float acc = 0;
  for (int k0 = 0; k0 < K; k0 += 16) {
    As[ty][tx] = b2f(A[row * K + k0 + tx]); Bs[ty][tx] = b2f(B[(k0 + ty) * N + col]);
    __syncthreads();
    for (int k = 0; k < 16; ++k) acc += As[ty][k] * Bs[k][tx];
    __syncthreads();
  }
  if (MODE == 1) acc += b2f(res[row * N + col]);
  if (MODE == 2) { acc += b2f(bias[col]); float u = 0.7978845608f * (acc + 0.044715f * acc * acc * acc); acc = 0.5f * acc * (1.f + tanhf(u)); }
  if (MODE == 3) acc += b2f(bias[col]) + b2f(res[row * N + col]);
  C[row * N + col] = f2b(acc);
}
__global__ void k_attn(const u16* qkv, u16* o) {
  int h = blockIdx.y, q = blockIdx.x * 64 + threadIdx.x;
  float qv[DH], acc[DH]; for (int d = 0; d < DH; ++d) { qv[d] = b2f(qkv[q * 3 * D + h * DH + d]) * 0.22360679775f; acc[d] = 0; }
  float m = -1e30f, l = 0;
  for (int k = 0; k < S; ++k) {
    float s = 0; for (int d = 0; d < DH; ++d) s += qv[d] * b2f(qkv[k * 3 * D + D + h * DH + d]);
    float mn = fmaxf(m, s), a = __expf(m - mn), p = __expf(s - mn); m = mn; l = l * a + p;
    for (int d = 0; d < DH; ++d) acc[d] = acc[d] * a + p * b2f(qkv[k * 3 * D + 2 * D + h * DH + d]);
  }
  for (int d = 0; d < DH; ++d) o[q * D + h * DH + d] = f2b(acc[d] / l);
}
extern "C" void kernel_launch(void* const* d_in, const int* in_sizes, int n_in, void* d_out, int out_size, void* d_ws, size_t ws_size, hipStream_t stream) {
  const u16* x = (const u16*)d_in[0]; const u16* wqkv = (const u16*)d_in[1]; const u16* wo = (const u16*)d_in[2];
  const u16* w1 = (const u16*)d_in[4]; const u16* b1 = (const u16*)d_in[5]; const u16* w2 = (const u16*)d_in[6]; const u16* b2 = (const u16*)d_in[7];
  u16* ws = (u16*)d_ws; u16* xn = ws; u16* qkv = xn + S * D; u16* o = qkv + S * 3 * D; u16* h = o + S * D; u16* hn = h + S * D; u16* g = hn + S * D;
  k_ln<<<S, 256, 0, stream>>>(x, xn);
  k_gemm<0><<<dim3(3 * D / 16, S / 16), 256, 0, stream>>>(xn, wqkv, qkv, nullptr, nullptr, 3 * D, D);
  k_attn<<<dim3(S / 64, H), 64, 0, stream>>>(qkv, o);
  k_gemm<1><<<dim3(D / 16, S / 16), 256, 0, stream>>>(o, wo, h, nullptr, x, D, D);
  k_ln<<<S, 256, 0, stream>>>(h, hn);
  k_gemm<2><<<dim3(F / 16, S / 16), 256, 0, stream>>>(hn, w1, g, b1, nullptr, F, D);
  k_gemm<3><<<dim3(D / 16, S / 16), 256, 0, stream>>>(g, w2, (u16*)d_out, b2, h, D, F);
}
```

```cpp
#include <hip/hip_runtime.h>
#include <hip/hip_bf16.h>

__global__ __launch_bounds__(256, 1) void mk_block(const void* x, const void* wqkv, const void* wo, const void* w1, const void* b1,
                                                   const void* w2, const void* b2, void* out, void* ws) {
  __shared__ __attribute__((aligned(16))) char lds[163840];
  asm volatile(
      R"ASM(
.Lmk_start:
s_load_dwordx16 s[52:67], s[0:1], 0
s_load_dwordx2 s[68:69], s[0:1], 64
s_getpc_b64 s[50:51]
.Lmk_pc:
v_lshrrev_b32 v4, 6, v0
v_and_b32 v0, 63, v0
v_readfirstlane_b32 s47, v4
s_and_b32 s48, s2, 7
s_lshr_b32 s49, s2, 3
v_lshlrev_b32 v2, 4, v0
v_lshlrev_b32 v3, 3, v0
v_mov_b32 v1, 0
s_cmp_lg_u32 s47, 0
s_cbranch_scc1 .Lmk_nopf_1
s_lshl_b32 s3, s49, 11
v_and_b32 v4, 15, v0
v_lshl_add_u32 v4, v4, 7, s3
v_min_u32 v4, .Lmk_end-.Lmk_pc-4, v4
global_load_dword v5, v4, s[50:51]
.Lmk_nopf_1:
s_mov_b32 s46, 100000
s_waitcnt lgkmcnt(0)
s_mov_b32 s4, s52
s_and_b32 s5, s53, 0xffff
s_mov_b32 s6, 2621440
s_mov_b32 s7, 0x20000
s_mov_b32 s8, s54
s_and_b32 s9, s55, 0xffff
s_mov_b32 s10, 2457600
s_mov_b32 s11, 0x20000
s_mov_b32 s12, s68
s_and_b32 s13, s69, 0xffff
s_mov_b32 s14, 31457280
s_mov_b32 s15, 0x20000
s_mov_b32 s20, s56
s_and_b32 s21, s57, 0xffff
s_mov_b32 s22, 819200
s_mov_b32 s23, 0x20000
s_mov_b32 s24, s58
s_and_b32 s25, s59, 0xffff
s_mov_b32 s26, 2211840
s_mov_b32 s27, 0x20000
s_mov_b32 s32, s60
s_and_b32 s33, s61, 0xffff
s_mov_b32 s34, 3456
s_mov_b32 s35, 0x20000
s_mov_b32 s28, s62
s_and_b32 s29, s63, 0xffff
s_mov_b32 s30, 2211840
s_mov_b32 s31, 0x20000
s_mov_b32 s36, s64
s_and_b32 s37, s65, 0xffff
s_mov_b32 s38, 1280
s_mov_b32 s39, 0x20000
s_mov_b32 s16, s66
s_and_b32 s17, s67, 0xffff
s_mov_b32 s18, 2621440
s_mov_b32 s19, 0x20000
buffer_load_dwordx2 v[6:7], off, s[12:15], 0 sc1
s_lshr_b32 s50, s48, 1
s_lshl_b32 s50, s50, 6
s_and_b32 s51, s48, 1
s_lshl_b32 s51, s51, 5
s_add_u32 s50, s50, s51
s_add_u32 s50, s50, s49
s_lshl_b32 s50, s50, 2
s_add_u32 s50, s50, s47
s_lshl_b32 s70, s50, 1
s_mul_i32 s50, s70, 1280
v_lshrrev_b32 v8, 5, v0
v_mul_u32_u24 v8, 1280, v8
v_and_b32 v9, 31, v0
v_lshl_add_u32 v10, v9, 4, v8
v_lshl_add_u32 v9, v9, 3, v8
buffer_load_dwordx4 v[244:247], v10, s[4:7], s50 offen
buffer_load_dwordx4 v[248:251], v10, s[4:7], s50 offen offset:512
buffer_load_dwordx2 v[252:253], v9, s[4:7], s50 offen offset:1024
s_lshr_b32 s50, s48, 1
s_lshl_b32 s50, s50, 2
s_lshr_b32 s51, s49, 3
s_and_b32 s52, s49, 7
s_mul_i32 s53, s49, 37
s_lshr_b32 s53, s53, 8
s_mul_i32 s54, s53, 7
s_sub_u32 s54, s49, s54
s_bitcmp1_b32 s48, 0
s_cselect_b32 s51, s53, s51
s_cselect_b32 s52, s54, s52
s_cselect_b32 s55, 8, 0
s_cmp_ge_u32 s51, 4
s_cbranch_scc1 .Lmk_p1skip_2
s_add_u32 s86, s50, s51
s_add_u32 s87, s52, s55
s_mul_i32 s56, s51, 5
s_add_u32 s56, s56, 1
s_lshr_b32 s56, s56, 1
s_add_u32 s88, s56, s52
s_cmp_ge_u32 s88, 10
s_cselect_b32 s56, 10, 0
s_sub_u32 s88, s88, s56
s_lshr_b32 s93, s47, 1
s_and_b32 s94, s47, 1
v_lshrrev_b32 v16, 3, v0
v_lshrrev_b32 v17, 4, v0
v_and_b32 v18, 7, v0
v_and_b32 v19, 15, v0
s_lshl_b32 s50, s86, 7
s_lshl_b32 s51, s47, 5
s_add_u32 s50, s50, s51
s_mul_i32 s50, s50, 1280
v_mul_u32_u24 v20, 1280, v16
v_add_u32 v20, s50, v20
v_add_u32 v21, 0, v17
v_xor_b32 v21, v21, v18
v_lshl_add_u32 v8, v21, 4, v20
v_add_u32 v21, 4, v17
v_xor_b32 v21, v21, v18
v_lshl_add_u32 v9, v21, 4, v20
v_add_u32 v9, 9216, v9
v_add_u32 v21, 0, v17
v_xor_b32 v21, v21, v18
v_lshl_add_u32 v10, v21, 4, v20
v_add_u32 v10, 18432, v10
v_add_u32 v21, 4, v17
v_xor_b32 v21, v21, v18
v_lshl_add_u32 v11, v21, 4, v20
v_add_u32 v11, 27648, v11
s_lshl_b32 s50, s47, 4
s_mul_i32 s50, s50, 3840
s_lshl_b32 s51, s87, 8
s_add_u32 s50, s50, s51
v_mul_u32_u24 v20, 3840, v17
v_add_u32 v20, s50, v20
v_lshlrev_b32 v21, 2, v17
v_xor_b32 v21, v21, v19
v_lshl_add_u32 v12, v21, 4, v20
v_add_u32 v13, 14336, v12
v_add_u32 v14, 28672, v12
v_add_u32 v15, 43008, v12
s_lshl_b32 s92, s47, 12
s_xor_b32 s67, s92, 0x10000
s_lshl_b32 s90, s88, 7
s_mul_i32 s91, s88, 245760
s_add_u32 s88, s88, 1
s_cmp_eq_u32 s88, 10
s_cselect_b32 s88, 0, s88
s_add_u32 m0, s92, 0
s_nop 0
buffer_load_dwordx4 v8, s[4:7], s90 offen offset:0 lds
buffer_load_dwordx4 v9, s[4:7], s90 offen offset:1024 lds
buffer_load_dwordx4 v10, s[4:7], s90 offen offset:2048 lds
buffer_load_dwordx4 v11, s[4:7], s90 offen offset:3072 lds
s_add_u32 m0, s92, 16384
s_nop 0
buffer_load_dwordx4 v12, s[8:11], s91 offen offset:0 lds
buffer_load_dwordx4 v13, s[8:11], s91 offen offset:1024 lds
buffer_load_dwordx4 v14, s[8:11], s91 offen offset:2048 lds
buffer_load_dwordx4 v15, s[8:11], s91 offen offset:3072 lds
s_mov_b32 s89, 8
v_lshrrev_b32 v34, 5, v0
v_and_b32 v35, 31, v0
v_bfe_u32 v16, v0, 1, 3
s_lshl_b32 s50, s93, 1
s_add_u32 s50, s50, s94
s_lshl_b32 s50, s50, 12
v_lshl_add_u32 v17, v35, 7, s50
v_add_u32 v18, 0, v34
v_xor_b32 v18, v18, v16
v_lshl_add_u32 v24, v18, 4, v17
v_xor_b32 v28, 4096, v24
v_add_u32 v18, 2, v34
v_xor_b32 v18, v18, v16
v_lshl_add_u32 v25, v18, 4, v17
s_lshl_b32 s90, s88, 7
s_mul_i32 s91, s88, 245760
s_add_u32 s88, s88, 1
s_cmp_eq_u32 s88, 10
s_cselect_b32 s88, 0, s88
s_add_u32 m0, s92, 32768
s_nop 0
buffer_load_dwordx4 v8, s[4:7], s90 offen offset:0 lds
v_xor_b32 v29, 4096, v25
v_add_u32 v18, 4, v34
v_xor_b32 v18, v18, v16
v_lshl_add_u32 v26, v18, 4, v17
v_xor_b32 v30, 4096, v26
v_add_u32 v18, 6, v34
v_xor_b32 v18, v18, v16
v_lshl_add_u32 v27, v18, 4, v17
v_xor_b32 v31, 4096, v27
v_bfe_u32 v16, v0, 2, 2
v_lshl_add_u32 v17, v34, 3, v16
v_lshlrev_b32 v17, 8, v17
s_mul_i32 s50, s87, 52
s_lshr_b32 s95, s50, 8
buffer_load_dwordx4 v9, s[4:7], s90 offen offset:1024 lds
s_mul_i32 s50, s95, 5
s_sub_u32 s96, s87, s50
v_bfe_u32 v18, v0, 1, 1
v_and_b32 v19, 1, v0
v_lshl_add_u32 v18, v19, 1, v18
v_and_b32 v19, 3, v0
s_cmp_eq_u32 s95, 2
s_cselect_b64 vcc, -1, 0
v_cndmask_b32 v19, v18, v19, vcc
v_bfe_u32 v18, v0, 4, 1
v_lshl_add_u32 v18, v18, 2, v19
v_lshl_add_u32 v17, v18, 3, v17
v_add_u32 v17, 16384, v17
s_lshl_b32 s50, s94, 1
buffer_load_dwordx4 v10, s[4:7], s90 offen offset:2048 lds
s_add_u32 s50, s50, s93
v_xor_b32 v18, s50, v16
v_lshl_add_u32 v32, v18, 6, v17
v_xor_b32 v33, 64, v32
v_bfe_u32 v16, v0, 2, 1
v_bfe_u32 v17, v0, 4, 1
v_xor_b32 v16, v16, v17
v_and_b32 v17, 3, v0
v_or_b32 v16, v16, v17
v_cmp_eq_u32 vcc, 0, v16
v_mov_b32 v17, 0x3f803f80
s_nop 0
v_cndmask_b32 v36, 0, v17, vcc
v_mov_b32 v37, v36
buffer_load_dwordx4 v11, s[4:7], s90 offen offset:3072 lds
v_mov_b32 v38, v36
v_mov_b32 v39, v36
v_mov_b32 v44, 0
v_mov_b32 v45, 0
v_mov_b32 v46, 0
v_mov_b32 v47, 0
v_mov_b32 v64, 0
v_mov_b32 v65, 0
v_mov_b32 v66, 0
v_mov_b32 v67, 0
v_mov_b32 v68, 0
v_mov_b32 v69, 0
v_mov_b32 v70, 0
v_mov_b32 v71, 0
s_add_u32 m0, s92, 49152
s_nop 0
buffer_load_dwordx4 v12, s[8:11], s91 offen offset:0 lds
v_mov_b32 v72, 0
v_mov_b32 v73, 0
v_mov_b32 v74, 0
v_mov_b32 v75, 0
v_mov_b32 v76, 0
v_mov_b32 v77, 0
v_mov_b32 v78, 0
v_mov_b32 v79, 0
v_mov_b32 v80, 0
v_mov_b32 v81, 0
v_mov_b32 v82, 0
v_mov_b32 v83, 0
v_mov_b32 v84, 0
v_mov_b32 v85, 0
buffer_load_dwordx4 v13, s[8:11], s91 offen offset:1024 lds
v_mov_b32 v86, 0
v_mov_b32 v87, 0
v_mov_b32 v88, 0
v_mov_b32 v89, 0
v_mov_b32 v90, 0
v_mov_b32 v91, 0
v_mov_b32 v92, 0
v_mov_b32 v93, 0
v_mov_b32 v94, 0
v_mov_b32 v95, 0
v_mov_b32 v96, 0
v_mov_b32 v97, 0
v_mov_b32 v98, 0
v_mov_b32 v99, 0
buffer_load_dwordx4 v14, s[8:11], s91 offen offset:2048 lds
v_mov_b32 v100, 0
v_mov_b32 v101, 0
v_mov_b32 v102, 0
v_mov_b32 v103, 0
v_mov_b32 v104, 0
v_mov_b32 v105, 0
v_mov_b32 v106, 0
v_mov_b32 v107, 0
v_mov_b32 v108, 0
v_mov_b32 v109, 0
v_mov_b32 v110, 0
v_mov_b32 v111, 0
v_mov_b32 v112, 0
v_mov_b32 v113, 0
buffer_load_dwordx4 v15, s[8:11], s91 offen offset:3072 lds
v_mov_b32 v114, 0
v_mov_b32 v115, 0
v_mov_b32 v116, 0
v_mov_b32 v117, 0
v_mov_b32 v118, 0
v_mov_b32 v119, 0
v_mov_b32 v120, 0
v_mov_b32 v121, 0
v_mov_b32 v122, 0
v_mov_b32 v123, 0
v_mov_b32 v124, 0
v_mov_b32 v125, 0
v_mov_b32 v126, 0
v_mov_b32 v127, 0
s_lshl_b32 s50, s94, 1
s_add_u32 s50, s50, s93
s_lshl_b32 s51, s96, 2
s_add_u32 s50, s50, s51
s_cmp_eq_u32 s95, 2
s_cbranch_scc1 .Lmk_p1v0_4
s_lshl_b32 s50, s50, 11
s_cmp_eq_u32 s95, 0
s_mov_b32 s61, 82048
s_cselect_b32 s61, 40960, s61
s_mov_b32 s62, 4718592
s_cselect_b32 s62, 2097152, s62
v_bfe_u32 v16, v0, 2, 1
v_bfe_u32 v17, v0, 3, 1
v_xor_b32 v16, v16, v17
v_mul_u32_u24 v16, 12, v16
v_xor_b32 v16, v16, v35
s_cselect_b64 vcc, -1, 0
v_cndmask_b32 v16, v16, v35, vcc
v_lshl_add_u32 v16, v16, 4, s50
v_lshl_add_u32 v160, v34, 9, v16
v_xor_b32 v161, 2048, v160
s_branch .Lmk_p1s_5
.Lmk_p1v0_4:
s_mov_b32 s61, 82048
s_mov_b32 s62, 4759552
s_lshl_b32 s50, s50, 9
v_lshl_add_u32 v16, v35, 4, s50
v_xor_b32 v17, 512, v16
v_mul_u32_u24 v18, 10240, v34
v_add_u32 v160, v16, v18
v_add_u32 v161, v17, v18
v_add_u32 v162, 20480, v160
v_add_u32 v163, 20480, v161
.Lmk_p1s_5:
s_lshl_b32 s54, s86, 2
s_lshl_b32 s55, s93, 1
s_add_u32 s54, s54, s55
s_xor_b32 s55, s94, 0
s_add_u32 s54, s54, s55
s_mul_i32 s54, s54, s61
s_add_u32 s63, s54, s62
s_lshl_b32 s54, s86, 2
s_lshl_b32 s55, s93, 1
s_add_u32 s54, s54, s55
s_xor_b32 s55, s94, 1
s_add_u32 s54, s54, s55
s_mul_i32 s54, s54, s61
s_add_u32 s64, s54, s62
s_lshl_b32 s54, s86, 1
s_add_u32 s54, s54, s93
s_mul_i32 s54, s54, 30
s_lshl_b32 s55, s87, 1
s_add_u32 s54, s54, s55
s_add_u32 s54, s54, s94
s_lshl_b32 s54, s54, 7
s_add_u32 s65, s54, 4096
s_lshl_b32 s54, s86, 2
s_lshl_b32 s55, s93, 1
s_add_u32 s54, s54, s55
s_add_u32 s54, s54, s94
s_lshl_b32 s54, s54, 9
s_add_u32 s54, s54, 1507328
v_lshl_add_u32 v59, v35, 4, s54
s_lshl_b32 s50, s2, 2
s_add_u32 s50, s50, s47
s_mul_i32 s59, s47, 5120
s_add_u32 s59, s59, 139264
s_cmp_ge_u32 s50, 592
s_cbranch_scc1 .Lmk_wt1_6
s_lshl_b32 s72, s50, 7
s_add_u32 s72, s72, 131072
s_cmp_ge_u32 s50, 160
s_cbranch_scc1 .Lmk_wt2_7
s_mov_b32 s40, s20
s_mov_b32 s41, s21
s_mov_b32 s42, s22
s_mov_b32 s43, s23
s_mul_i32 s71, s50, 5120
s_add_u32 s71, s71, 24117248
s_mul_i32 s55, s50, 3277
s_lshr_b32 s55, s55, 16
s_mul_i32 s57, s55, 20
s_sub_u32 s57, s50, s57
s_mul_i32 s58, s57, 205
s_lshr_b32 s58, s58, 10
s_lshr_b32 s56, s58, 1
s_mul_i32 s51, s58, 5
s_sub_u32 s57, s57, s51
s_mul_i32 s51, s58, 160
s_mul_i32 s58, s57, 40
s_cmp_eq_u32 s57, 4
s_cselect_b32 s58, 16, s58
s_mov_b32 s52, 20
s_cselect_b32 s52, 80, s52
s_cselect_b32 s53, 40, 4
s_cselect_b32 s54, 20, 8
)ASM"
      R"ASM(
s_add_u32 s51, s51, s58
s_branch .Lmk_wt3_8
.Lmk_wt2_7:
s_mov_b32 s40, s28
s_mov_b32 s41, s29
s_mov_b32 s42, s30
s_mov_b32 s43, s31
s_sub_u32 s57, s50, 160
s_mul_i32 s71, s57, 5120
s_add_u32 s71, s71, 25165824
s_mul_i32 s55, s57, 1214
s_lshr_b32 s55, s55, 16
s_mul_i32 s58, s55, 54
s_sub_u32 s57, s57, s58
s_lshl_b32 s51, s57, 5
s_cmp_ge_u32 s57, 28
s_cselect_b32 s56, 1, 0
s_mov_b32 s52, 16
s_mov_b32 s53, 4
s_mov_b32 s54, 8
.Lmk_wt3_8:
s_mul_i32 s55, s55, 160
s_mul_i32 s51, s51, 1280
s_add_u32 s51, s51, s55
v_add_u32 v16, 0, v0
v_mul_u32_u24 v17, 6554, v16
v_lshrrev_b32 v17, 16, v17
v_mul_u32_u24 v18, 10, v17
v_sub_u32 v16, v16, v18
v_and_b32 v18, 3, v17
v_bfe_u32 v19, v17, 2, 1
v_mad_u32_u24 v18, v19, s54, v18
v_bfe_u32 v19, v17, 3, 1
v_mad_u32_u24 v18, v19, s53, v18
v_lshrrev_b32 v19, 4, v17
v_mad_u32_u24 v18, v19, s52, v18
v_mul_u32_u24 v18, 1280, v18
v_lshl_add_u32 v18, v16, 4, v18
s_sub_u32 s57, s51, 0
v_add_u32 v48, s57, v18
v_add_u32 v16, 64, v0
v_mul_u32_u24 v17, 6554, v16
v_lshrrev_b32 v17, 16, v17
v_mul_u32_u24 v18, 10, v17
v_sub_u32 v16, v16, v18
v_and_b32 v18, 3, v17
v_bfe_u32 v19, v17, 2, 1
v_mad_u32_u24 v18, v19, s54, v18
v_bfe_u32 v19, v17, 3, 1
v_mad_u32_u24 v18, v19, s53, v18
v_lshrrev_b32 v19, 4, v17
v_mad_u32_u24 v18, v19, s52, v18
v_mul_u32_u24 v18, 1280, v18
v_lshl_add_u32 v18, v16, 4, v18
s_sub_u32 s57, s51, 1024
v_add_u32 v49, s57, v18
v_add_u32 v16, 128, v0
v_mul_u32_u24 v17, 6554, v16
v_lshrrev_b32 v17, 16, v17
v_mul_u32_u24 v18, 10, v17
v_sub_u32 v16, v16, v18
v_and_b32 v18, 3, v17
v_bfe_u32 v19, v17, 2, 1
v_mad_u32_u24 v18, v19, s54, v18
v_bfe_u32 v19, v17, 3, 1
v_mad_u32_u24 v18, v19, s53, v18
v_lshrrev_b32 v19, 4, v17
v_mad_u32_u24 v18, v19, s52, v18
v_mul_u32_u24 v18, 1280, v18
v_lshl_add_u32 v18, v16, 4, v18
s_sub_u32 s57, s51, 2048
v_add_u32 v50, s57, v18
v_add_u32 v16, 192, v0
v_mul_u32_u24 v17, 6554, v16
v_lshrrev_b32 v17, 16, v17
v_mul_u32_u24 v18, 10, v17
v_sub_u32 v16, v16, v18
v_and_b32 v18, 3, v17
v_bfe_u32 v19, v17, 2, 1
v_mad_u32_u24 v18, v19, s54, v18
v_bfe_u32 v19, v17, 3, 1
v_mad_u32_u24 v18, v19, s53, v18
v_lshrrev_b32 v19, 4, v17
v_mad_u32_u24 v18, v19, s52, v18
v_mul_u32_u24 v18, 1280, v18
v_lshl_add_u32 v18, v16, 4, v18
s_sub_u32 s57, s51, 3072
v_add_u32 v51, s57, v18
v_add_u32 v16, 256, v0
v_mul_u32_u24 v17, 6554, v16
v_lshrrev_b32 v17, 16, v17
v_mul_u32_u24 v18, 10, v17
v_sub_u32 v16, v16, v18
v_and_b32 v18, 3, v17
v_bfe_u32 v19, v17, 2, 1
v_mad_u32_u24 v18, v19, s54, v18
v_bfe_u32 v19, v17, 3, 1
v_mad_u32_u24 v18, v19, s53, v18
v_lshrrev_b32 v19, 4, v17
v_mad_u32_u24 v18, v19, s52, v18
v_mul_u32_u24 v18, 1280, v18
v_lshl_add_u32 v18, v16, 4, v18
s_sub_u32 s57, s51, 0
v_add_u32 v52, s57, v18
v_lshrrev_b32 v16, 4, v0
v_bfe_u32 v17, v0, 2, 2
v_lshl_add_u32 v16, v16, 3, v17
v_mul_u32_u24 v16, 160, v16
v_add_u32 v16, s59, v16
v_and_b32 v17, 3, v0
v_lshl_add_u32 v18, v17, 4, v16
s_lshl_b32 s56, s56, 6
s_xor_b32 s57, s56, 64
v_add_u32 v53, s56, v18
v_add_u32 v55, 8, v53
v_add_u32 v57, s57, v18
v_add_u32 v40, 8, v57
v_lshl_add_u32 v42, v17, 3, v16
v_add_u32 v42, 128, v42
v_add_u32 v54, 640, v53
v_add_u32 v56, 640, v55
v_add_u32 v58, 640, v57
v_add_u32 v41, 640, v40
v_add_u32 v43, 640, v42
s_branch .Lmk_wtd_9
.Lmk_wt1_6:
s_mov_b32 s40, s24
s_mov_b32 s41, s25
s_mov_b32 s42, s26
s_mov_b32 s43, s27
s_sub_u32 s51, s50, 592
s_lshr_b32 s52, s51, 4
s_lshl_b32 s52, s52, 1
s_and_b32 s53, s51, 1
s_add_u32 s52, s52, s53
s_bfe_u32 s53, s51, 0x30001
s_lshl_b32 s54, s52, 3
s_add_u32 s54, s54, s53
s_mul_i32 s71, s54, 5120
s_add_u32 s71, s71, 28311552
s_add_u32 s54, s54, 592
s_lshl_b32 s72, s54, 7
s_add_u32 s72, s72, 131072
s_mul_i32 s53, s53, 276480
s_lshl_b32 s52, s52, 6
s_add_u32 s53, s53, s52
v_lshrrev_b32 v16, 2, v0
v_mul_u32_u24 v16, 3456, v16
v_and_b32 v17, 3, v0
v_lshl_add_u32 v16, v17, 4, v16
v_add_u32 v48, s53, v16
v_add_u32 v49, 54272, v48
v_add_u32 v50, 108544, v48
v_add_u32 v51, 162816, v48
v_add_u32 v52, 221184, v48
v_lshrrev_b32 v16, 5, v0
v_bfe_u32 v18, v0, 2, 2
v_lshl_add_u32 v16, v16, 3, v18
v_lshlrev_b32 v16, 6, v16
v_bfe_u32 v18, v0, 4, 1
v_lshl_add_u32 v16, v18, 5, v16
v_lshl_add_u32 v16, v17, 3, v16
v_add_u32 v53, s59, v16
v_add_u32 v54, 256, v53
v_add_u32 v55, 1024, v53
v_add_u32 v56, 1280, v53
v_add_u32 v57, 2048, v53
v_add_u32 v58, 2304, v53
v_add_u32 v40, 3072, v53
v_add_u32 v41, 3328, v53
v_add_u32 v42, 4096, v53
v_add_u32 v43, 4352, v53
.Lmk_wtd_9:
s_waitcnt vmcnt(16)
v_readfirstlane_b32 s44, v6
v_readfirstlane_b32 s45, v7
s_add_u32 s44, s44, 1
s_addc_u32 s45, s45, 0
v_mov_b32 v236, 0
v_mov_b32 v237, 0
v_mov_b32 v238, 0x3f803f80
v_dot2c_f32_bf16 v236, v244, v238
v_dot2c_f32_bf16 v237, v244, v244
v_dot2c_f32_bf16 v236, v245, v238
v_dot2c_f32_bf16 v237, v245, v245
v_dot2c_f32_bf16 v236, v246, v238
v_dot2c_f32_bf16 v237, v246, v246
v_dot2c_f32_bf16 v236, v247, v238
v_dot2c_f32_bf16 v237, v247, v247
v_dot2c_f32_bf16 v236, v248, v238
v_dot2c_f32_bf16 v237, v248, v248
v_dot2c_f32_bf16 v236, v249, v238
v_dot2c_f32_bf16 v237, v249, v249
v_dot2c_f32_bf16 v236, v250, v238
v_dot2c_f32_bf16 v237, v250, v250
v_dot2c_f32_bf16 v236, v251, v238
v_dot2c_f32_bf16 v237, v251, v251
v_dot2c_f32_bf16 v236, v252, v238
v_dot2c_f32_bf16 v237, v252, v252
v_dot2c_f32_bf16 v236, v253, v238
v_dot2c_f32_bf16 v237, v253, v253
s_nop 1
s_nop 0
v_add_f32_dpp v236, v236, v236 row_ror:8 row_mask:0xf bank_mask:0xf
v_add_f32_dpp v237, v237, v237 row_ror:8 row_mask:0xf bank_mask:0xf
s_nop 0
v_add_f32_dpp v236, v236, v236 row_ror:4 row_mask:0xf bank_mask:0xf
v_add_f32_dpp v237, v237, v237 row_ror:4 row_mask:0xf bank_mask:0xf
s_nop 0
v_add_f32_dpp v236, v236, v236 row_ror:2 row_mask:0xf bank_mask:0xf
v_add_f32_dpp v237, v237, v237 row_ror:2 row_mask:0xf bank_mask:0xf
s_nop 0
v_add_f32_dpp v236, v236, v236 row_ror:1 row_mask:0xf bank_mask:0xf
v_add_f32_dpp v237, v237, v237 row_ror:1 row_mask:0xf bank_mask:0xf
v_mov_b32 v240, v236
v_mov_b32 v241, v237
s_nop 0
v_permlane16_swap_b32 v236, v240
v_permlane16_swap_b32 v237, v241
v_add_f32 v236, v236, v240
v_add_f32 v237, v237, v241
v_mul_f32 v240, 0xbacccccd, v236
v_mul_f32 v237, 0x3acccccd, v237
v_fma_f32 v237, -v240, v240, v237
v_max_f32 v237, 0, v237
v_add_f32 v237, 0x3727c5ac, v237
v_rsq_f32 v242, v237
v_mov_b32 v241, s44
v_mov_b32 v243, s45
s_lshl_b32 s50, s70, 4
s_add_u32 s50, s50, 1507328
v_lshrrev_b32 v236, 1, v0
s_mov_b32 exec_lo, 1
s_mov_b32 exec_hi, 1
buffer_store_dwordx4 v[240:243], v236, s[12:15], s50 offen sc1
s_mov_b64 exec, -1
s_waitcnt vmcnt(9)
s_barrier
ds_read_b64_tr_b16 v[136:137], v32 offset:0
ds_read_b64_tr_b16 v[138:139], v32 offset:1024
ds_read_b128 v[128:131], v24 offset:0
ds_read_b64_tr_b16 v[140:141], v33 offset:0
ds_read_b64_tr_b16 v[142:143], v33 offset:1024
ds_read_b128 v[132:135], v28 offset:0
s_mov_b32 s97, 5
s_cmp_eq_u32 s95, 2
s_cbranch_scc1 .Lmk_p1vloop_10
.Lmk_p1top_12:
s_waitcnt lgkmcnt(0)
v_mfma_f32_32x32x16_bf16 v[64:79], v[136:139], v[128:131], v[64:79]
ds_read_b64_tr_b16 v[152:153], v32 offset:4096
ds_read_b64_tr_b16 v[154:155], v32 offset:5120
ds_read_b128 v[144:147], v25 offset:0
s_lshl_b32 s90, s88, 7
s_mul_i32 s91, s88, 245760
s_add_u32 s88, s88, 1
v_mfma_f32_32x32x16_bf16 v[80:95], v[140:143], v[128:131], v[80:95]
ds_read_b64_tr_b16 v[156:157], v33 offset:4096
ds_read_b64_tr_b16 v[158:159], v33 offset:5120
ds_read_b128 v[148:151], v29 offset:0
s_cmp_eq_u32 s88, 10
s_cselect_b32 s88, 0, s88
s_sub_u32 s89, s89, 1
v_mfma_f32_32x32x16_bf16 v[96:111], v[136:139], v[132:135], v[96:111]
s_cmp_lt_i32 s89, 0
s_cselect_b32 s90, 0x40000000, s90
s_cselect_b32 s91, 0x40000000, s91
s_add_u32 m0, s67, 0
v_mfma_f32_32x32x16_bf16 v[112:127], v[140:143], v[132:135], v[112:127]
buffer_load_dwordx4 v8, s[4:7], s90 offen offset:0 lds
v_mfma_f32_16x16x32_bf16 v[44:47], v[36:39], v[136:139], v[44:47]
buffer_load_dwordx4 v9, s[4:7], s90 offen offset:1024 lds
s_waitcnt lgkmcnt(0)
v_mfma_f32_32x32x16_bf16 v[64:79], v[152:155], v[144:147], v[64:79]
ds_read_b64_tr_b16 v[136:137], v32 offset:8192
ds_read_b64_tr_b16 v[138:139], v32 offset:9216
ds_read_b128 v[128:131], v26 offset:0
v_mfma_f32_32x32x16_bf16 v[80:95], v[156:159], v[144:147], v[80:95]
ds_read_b64_tr_b16 v[140:141], v33 offset:8192
ds_read_b64_tr_b16 v[142:143], v33 offset:9216
ds_read_b128 v[132:135], v30 offset:0
v_mfma_f32_32x32x16_bf16 v[96:111], v[152:155], v[148:151], v[96:111]
v_mfma_f32_32x32x16_bf16 v[112:127], v[156:159], v[148:151], v[112:127]
buffer_load_dwordx4 v10, s[4:7], s90 offen offset:2048 lds
v_mfma_f32_16x16x32_bf16 v[44:47], v[36:39], v[152:155], v[44:47]
buffer_load_dwordx4 v11, s[4:7], s90 offen offset:3072 lds
s_waitcnt lgkmcnt(0)
v_mfma_f32_32x32x16_bf16 v[64:79], v[136:139], v[128:131], v[64:79]
ds_read_b64_tr_b16 v[152:153], v32 offset:12288
ds_read_b64_tr_b16 v[154:155], v32 offset:13312
ds_read_b128 v[144:147], v27 offset:0
v_mfma_f32_32x32x16_bf16 v[80:95], v[140:143], v[128:131], v[80:95]
ds_read_b64_tr_b16 v[156:157], v33 offset:12288
ds_read_b64_tr_b16 v[158:159], v33 offset:13312
ds_read_b128 v[148:151], v31 offset:0
v_mfma_f32_32x32x16_bf16 v[96:111], v[136:139], v[132:135], v[96:111]
s_add_u32 m0, s67, 16384
v_mfma_f32_32x32x16_bf16 v[112:127], v[140:143], v[132:135], v[112:127]
buffer_load_dwordx4 v12, s[8:11], s91 offen offset:0 lds
v_mfma_f32_16x16x32_bf16 v[44:47], v[36:39], v[136:139], v[44:47]
buffer_load_dwordx4 v13, s[8:11], s91 offen offset:1024 lds
s_waitcnt vmcnt(6) lgkmcnt(0)
s_barrier
v_mfma_f32_32x32x16_bf16 v[64:79], v[152:155], v[144:147], v[64:79]
ds_read_b64_tr_b16 v[136:137], v32 offset:32768
ds_read_b64_tr_b16 v[138:139], v32 offset:33792
ds_read_b128 v[128:131], v24 offset:32768
v_mfma_f32_32x32x16_bf16 v[80:95], v[156:159], v[144:147], v[80:95]
ds_read_b64_tr_b16 v[140:141], v33 offset:32768
ds_read_b64_tr_b16 v[142:143], v33 offset:33792
ds_read_b128 v[132:135], v28 offset:32768
v_mfma_f32_32x32x16_bf16 v[96:111], v[152:155], v[148:151], v[96:111]
buffer_load_dwordx4 v[60:63], v59, s[12:15], 0 offen sc1
v_mfma_f32_32x32x16_bf16 v[112:127], v[156:159], v[148:151], v[112:127]
buffer_load_dwordx4 v14, s[8:11], s91 offen offset:2048 lds
v_mfma_f32_16x16x32_bf16 v[44:47], v[36:39], v[152:155], v[44:47]
buffer_load_dwordx4 v15, s[8:11], s91 offen offset:3072 lds
s_waitcnt lgkmcnt(0)
v_mfma_f32_32x32x16_bf16 v[64:79], v[136:139], v[128:131], v[64:79]
ds_read_b64_tr_b16 v[152:153], v32 offset:36864
ds_read_b64_tr_b16 v[154:155], v32 offset:37888
ds_read_b128 v[144:147], v25 offset:32768
s_lshl_b32 s90, s88, 7
s_mul_i32 s91, s88, 245760
s_add_u32 s88, s88, 1
v_mfma_f32_32x32x16_bf16 v[80:95], v[140:143], v[128:131], v[80:95]
ds_read_b64_tr_b16 v[156:157], v33 offset:36864
ds_read_b64_tr_b16 v[158:159], v33 offset:37888
ds_read_b128 v[148:151], v29 offset:32768
s_cmp_eq_u32 s88, 10
s_cselect_b32 s88, 0, s88
s_sub_u32 s89, s89, 1
v_mfma_f32_32x32x16_bf16 v[96:111], v[136:139], v[132:135], v[96:111]
s_cmp_lt_i32 s89, 0
s_cselect_b32 s90, 0x40000000, s90
s_cselect_b32 s91, 0x40000000, s91
s_add_u32 m0, s67, 32768
v_mfma_f32_32x32x16_bf16 v[112:127], v[140:143], v[132:135], v[112:127]
buffer_load_dwordx4 v8, s[4:7], s90 offen offset:0 lds
v_mfma_f32_16x16x32_bf16 v[44:47], v[36:39], v[136:139], v[44:47]
buffer_load_dwordx4 v9, s[4:7], s90 offen offset:1024 lds
s_waitcnt lgkmcnt(0)
v_mfma_f32_32x32x16_bf16 v[64:79], v[152:155], v[144:147], v[64:79]
ds_read_b64_tr_b16 v[136:137], v32 offset:40960
ds_read_b64_tr_b16 v[138:139], v32 offset:41984
ds_read_b128 v[128:131], v26 offset:32768
v_mfma_f32_32x32x16_bf16 v[80:95], v[156:159], v[144:147], v[80:95]
ds_read_b64_tr_b16 v[140:141], v33 offset:40960
ds_read_b64_tr_b16 v[142:143], v33 offset:41984
ds_read_b128 v[132:135], v30 offset:32768
v_mfma_f32_32x32x16_bf16 v[96:111], v[152:155], v[148:151], v[96:111]
v_mfma_f32_32x32x16_bf16 v[112:127], v[156:159], v[148:151], v[112:127]
buffer_load_dwordx4 v10, s[4:7], s90 offen offset:2048 lds
v_mfma_f32_16x16x32_bf16 v[44:47], v[36:39], v[152:155], v[44:47]
buffer_load_dwordx4 v11, s[4:7], s90 offen offset:3072 lds
s_waitcnt lgkmcnt(0)
v_mfma_f32_32x32x16_bf16 v[64:79], v[136:139], v[128:131], v[64:79]
ds_read_b64_tr_b16 v[152:153], v32 offset:45056
ds_read_b64_tr_b16 v[154:155], v32 offset:46080
ds_read_b128 v[144:147], v27 offset:32768
v_mfma_f32_32x32x16_bf16 v[80:95], v[140:143], v[128:131], v[80:95]
ds_read_b64_tr_b16 v[156:157], v33 offset:45056
ds_read_b64_tr_b16 v[158:159], v33 offset:46080
ds_read_b128 v[148:151], v31 offset:32768
v_mfma_f32_32x32x16_bf16 v[96:111], v[136:139], v[132:135], v[96:111]
s_add_u32 m0, s67, 49152
v_mfma_f32_32x32x16_bf16 v[112:127], v[140:143], v[132:135], v[112:127]
buffer_load_dwordx4 v12, s[8:11], s91 offen offset:0 lds
v_mfma_f32_16x16x32_bf16 v[44:47], v[36:39], v[136:139], v[44:47]
buffer_load_dwordx4 v13, s[8:11], s91 offen offset:1024 lds
s_waitcnt vmcnt(6) lgkmcnt(0)
s_barrier
v_xor_b32 v24, 0x10000, v24
v_xor_b32 v28, 0x10000, v28
v_xor_b32 v32, 0x10000, v32
v_xor_b32 v33, 0x10000, v33
v_mfma_f32_32x32x16_bf16 v[64:79], v[152:155], v[144:147], v[64:79]
ds_read_b64_tr_b16 v[136:137], v32 offset:0
ds_read_b64_tr_b16 v[138:139], v32 offset:1024
ds_read_b128 v[128:131], v24 offset:0
v_xor_b32 v25, 0x10000, v25
v_xor_b32 v26, 0x10000, v26
v_xor_b32 v27, 0x10000, v27
v_mfma_f32_32x32x16_bf16 v[80:95], v[156:159], v[144:147], v[80:95]
ds_read_b64_tr_b16 v[140:141], v33 offset:0
ds_read_b64_tr_b16 v[142:143], v33 offset:1024
ds_read_b128 v[132:135], v28 offset:0
v_xor_b32 v29, 0x10000, v29
v_xor_b32 v30, 0x10000, v30
v_xor_b32 v31, 0x10000, v31
v_mfma_f32_32x32x16_bf16 v[96:111], v[152:155], v[148:151], v[96:111]
v_mfma_f32_32x32x16_bf16 v[112:127], v[156:159], v[148:151], v[112:127]
buffer_load_dwordx4 v14, s[8:11], s91 offen offset:2048 lds
v_mfma_f32_16x16x32_bf16 v[44:47], v[36:39], v[152:155], v[44:47]
buffer_load_dwordx4 v15, s[8:11], s91 offen offset:3072 lds
s_xor_b32 s92, s92, 0x10000
s_xor_b32 s67, s67, 0x10000
s_sub_u32 s97, s97, 1
s_cmp_lg_u32 s97, 0
s_cbranch_scc1 .Lmk_p1top_12
)ASM"
      R"ASM(
s_mul_i32 s59, s47, 5120
s_add_u32 m0, s59, 139264
s_nop 0
buffer_load_dwordx4 v48, s[40:43], 0 offen offset:0 lds
buffer_load_dwordx4 v49, s[40:43], 0 offen offset:1024 lds
buffer_load_dwordx4 v50, s[40:43], 0 offen offset:2048 lds
buffer_load_dwordx4 v51, s[40:43], 0 offen offset:3072 lds
s_add_u32 m0, s59, 143360
s_nop 0
buffer_load_dwordx4 v52, s[40:43], 0 offen lds
.Lmk_xs_13:
v_cmp_ne_u32 vcc, s44, v61
v_cmp_ne_u32 s[50:51], s45, v63
s_or_b64 vcc, vcc, s[50:51]
s_cbranch_vccz .Lmk_xsd_14
buffer_load_dwordx4 v[60:63], v59, s[12:15], 0 offen sc1
s_waitcnt vmcnt(0)
s_sub_u32 s46, s46, 1
s_cbranch_scc0 .Lmk_xs_13
s_mov_b32 s46, 0
.Lmk_xsd_14:
s_lshl_b32 s54, s93, 1
s_add_u32 s54, s54, s94
s_lshl_b32 s54, s54, 7
s_add_u32 s54, s54, 131072
v_lshl_add_u32 v16, v35, 2, s54
s_cmp_eq_u32 s95, 0
s_cselect_b32 s55, 0x3ea52b5c, 0x3f800000
v_mul_f32 v62, s55, v62
s_lshl_b32 s54, s94, 1
s_add_u32 s54, s54, s93
s_lshl_b32 s54, s54, 7
s_add_u32 s54, s54, 131072
v_lshl_add_u32 v17, v35, 2, s54
ds_write_b32 v16, v60
ds_write_b32 v16, v62 offset:512
ds_write_b32 v17, v44 offset:1024
s_waitcnt lgkmcnt(0)
s_barrier
v_xor_b32 v18, 128, v16
ds_read_b32 v168, v16
ds_read_b32 v172, v16 offset:512
ds_read_b32 v170, v18
ds_read_b32 v174, v18 offset:512
s_lshl_b32 s54, s94, 1
s_xor_b32 s55, s93, 0
s_add_u32 s54, s54, s55
s_lshl_b32 s54, s54, 7
s_add_u32 s54, s54, 132096
v_lshl_add_u32 v19, v34, 4, s54
ds_read_b128 v[176:179], v19 offset:0
ds_read_b128 v[180:183], v19 offset:32
ds_read_b128 v[184:187], v19 offset:64
ds_read_b128 v[188:191], v19 offset:96
s_lshl_b32 s54, s94, 1
s_xor_b32 s55, s93, 1
s_add_u32 s54, s54, s55
s_lshl_b32 s54, s54, 7
s_add_u32 s54, s54, 132096
v_lshl_add_u32 v19, v34, 4, s54
ds_read_b128 v[192:195], v19 offset:0
ds_read_b128 v[196:199], v19 offset:32
ds_read_b128 v[200:203], v19 offset:64
ds_read_b128 v[204:207], v19 offset:96
s_waitcnt lgkmcnt(0)
v_pk_fma_f32 v[64:65], v[176:177], v[168:169], v[64:65] op_sel_hi:[1,0,1]
v_pk_mul_f32 v[64:65], v[64:65], v[172:173] op_sel_hi:[1,0]
v_pk_fma_f32 v[66:67], v[178:179], v[168:169], v[66:67] op_sel_hi:[1,0,1]
v_pk_mul_f32 v[66:67], v[66:67], v[172:173] op_sel_hi:[1,0]
v_pk_fma_f32 v[68:69], v[180:181], v[168:169], v[68:69] op_sel_hi:[1,0,1]
v_pk_mul_f32 v[68:69], v[68:69], v[172:173] op_sel_hi:[1,0]
v_pk_fma_f32 v[70:71], v[182:183], v[168:169], v[70:71] op_sel_hi:[1,0,1]
v_pk_mul_f32 v[70:71], v[70:71], v[172:173] op_sel_hi:[1,0]
v_cvt_pk_bf16_f32 v64, v64, v65
v_cvt_pk_bf16_f32 v65, v66, v67
v_cvt_pk_bf16_f32 v66, v68, v69
v_cvt_pk_bf16_f32 v67, v70, v71
buffer_store_dwordx4 v[64:67], v160, s[12:15], s63 offen offset:0 sc1
v_pk_fma_f32 v[72:73], v[184:185], v[168:169], v[72:73] op_sel_hi:[1,0,1]
v_pk_mul_f32 v[72:73], v[72:73], v[172:173] op_sel_hi:[1,0]
v_pk_fma_f32 v[74:75], v[186:187], v[168:169], v[74:75] op_sel_hi:[1,0,1]
v_pk_mul_f32 v[74:75], v[74:75], v[172:173] op_sel_hi:[1,0]
v_pk_fma_f32 v[76:77], v[188:189], v[168:169], v[76:77] op_sel_hi:[1,0,1]
v_pk_mul_f32 v[76:77], v[76:77], v[172:173] op_sel_hi:[1,0]
v_pk_fma_f32 v[78:79], v[190:191], v[168:169], v[78:79] op_sel_hi:[1,0,1]
v_pk_mul_f32 v[78:79], v[78:79], v[172:173] op_sel_hi:[1,0]
v_cvt_pk_bf16_f32 v72, v72, v73
v_cvt_pk_bf16_f32 v73, v74, v75
v_cvt_pk_bf16_f32 v74, v76, v77
v_cvt_pk_bf16_f32 v75, v78, v79
buffer_store_dwordx4 v[72:75], v160, s[12:15], s63 offen offset:1024 sc1
v_pk_fma_f32 v[80:81], v[192:193], v[168:169], v[80:81] op_sel_hi:[1,0,1]
v_pk_mul_f32 v[80:81], v[80:81], v[172:173] op_sel_hi:[1,0]
v_pk_fma_f32 v[82:83], v[194:195], v[168:169], v[82:83] op_sel_hi:[1,0,1]
v_pk_mul_f32 v[82:83], v[82:83], v[172:173] op_sel_hi:[1,0]
v_pk_fma_f32 v[84:85], v[196:197], v[168:169], v[84:85] op_sel_hi:[1,0,1]
v_pk_mul_f32 v[84:85], v[84:85], v[172:173] op_sel_hi:[1,0]
v_pk_fma_f32 v[86:87], v[198:199], v[168:169], v[86:87] op_sel_hi:[1,0,1]
v_pk_mul_f32 v[86:87], v[86:87], v[172:173] op_sel_hi:[1,0]
v_cvt_pk_bf16_f32 v80, v80, v81
v_cvt_pk_bf16_f32 v81, v82, v83
v_cvt_pk_bf16_f32 v82, v84, v85
v_cvt_pk_bf16_f32 v83, v86, v87
buffer_store_dwordx4 v[80:83], v161, s[12:15], s63 offen offset:0 sc1
v_pk_fma_f32 v[88:89], v[200:201], v[168:169], v[88:89] op_sel_hi:[1,0,1]
v_pk_mul_f32 v[88:89], v[88:89], v[172:173] op_sel_hi:[1,0]
v_pk_fma_f32 v[90:91], v[202:203], v[168:169], v[90:91] op_sel_hi:[1,0,1]
v_pk_mul_f32 v[90:91], v[90:91], v[172:173] op_sel_hi:[1,0]
v_pk_fma_f32 v[92:93], v[204:205], v[168:169], v[92:93] op_sel_hi:[1,0,1]
v_pk_mul_f32 v[92:93], v[92:93], v[172:173] op_sel_hi:[1,0]
v_pk_fma_f32 v[94:95], v[206:207], v[168:169], v[94:95] op_sel_hi:[1,0,1]
v_pk_mul_f32 v[94:95], v[94:95], v[172:173] op_sel_hi:[1,0]
v_cvt_pk_bf16_f32 v88, v88, v89
v_cvt_pk_bf16_f32 v89, v90, v91
v_cvt_pk_bf16_f32 v90, v92, v93
v_cvt_pk_bf16_f32 v91, v94, v95
buffer_store_dwordx4 v[88:91], v161, s[12:15], s63 offen offset:1024 sc1
v_pk_fma_f32 v[96:97], v[176:177], v[170:171], v[96:97] op_sel_hi:[1,0,1]
v_pk_mul_f32 v[96:97], v[96:97], v[174:175] op_sel_hi:[1,0]
v_pk_fma_f32 v[98:99], v[178:179], v[170:171], v[98:99] op_sel_hi:[1,0,1]
v_pk_mul_f32 v[98:99], v[98:99], v[174:175] op_sel_hi:[1,0]
v_pk_fma_f32 v[100:101], v[180:181], v[170:171], v[100:101] op_sel_hi:[1,0,1]
v_pk_mul_f32 v[100:101], v[100:101], v[174:175] op_sel_hi:[1,0]
v_pk_fma_f32 v[102:103], v[182:183], v[170:171], v[102:103] op_sel_hi:[1,0,1]
v_pk_mul_f32 v[102:103], v[102:103], v[174:175] op_sel_hi:[1,0]
v_cvt_pk_bf16_f32 v96, v96, v97
v_cvt_pk_bf16_f32 v97, v98, v99
v_cvt_pk_bf16_f32 v98, v100, v101
v_cvt_pk_bf16_f32 v99, v102, v103
buffer_store_dwordx4 v[96:99], v160, s[12:15], s64 offen offset:0 sc1
v_pk_fma_f32 v[104:105], v[184:185], v[170:171], v[104:105] op_sel_hi:[1,0,1]
v_pk_mul_f32 v[104:105], v[104:105], v[174:175] op_sel_hi:[1,0]
v_pk_fma_f32 v[106:107], v[186:187], v[170:171], v[106:107] op_sel_hi:[1,0,1]
v_pk_mul_f32 v[106:107], v[106:107], v[174:175] op_sel_hi:[1,0]
v_pk_fma_f32 v[108:109], v[188:189], v[170:171], v[108:109] op_sel_hi:[1,0,1]
v_pk_mul_f32 v[108:109], v[108:109], v[174:175] op_sel_hi:[1,0]
v_pk_fma_f32 v[110:111], v[190:191], v[170:171], v[110:111] op_sel_hi:[1,0,1]
v_pk_mul_f32 v[110:111], v[110:111], v[174:175] op_sel_hi:[1,0]
v_cvt_pk_bf16_f32 v104, v104, v105
v_cvt_pk_bf16_f32 v105, v106, v107
v_cvt_pk_bf16_f32 v106, v108, v109
v_cvt_pk_bf16_f32 v107, v110, v111
buffer_store_dwordx4 v[104:107], v160, s[12:15], s64 offen offset:1024 sc1
v_pk_fma_f32 v[112:113], v[192:193], v[170:171], v[112:113] op_sel_hi:[1,0,1]
v_pk_mul_f32 v[112:113], v[112:113], v[174:175] op_sel_hi:[1,0]
v_pk_fma_f32 v[114:115], v[194:195], v[170:171], v[114:115] op_sel_hi:[1,0,1]
v_pk_mul_f32 v[114:115], v[114:115], v[174:175] op_sel_hi:[1,0]
v_pk_fma_f32 v[116:117], v[196:197], v[170:171], v[116:117] op_sel_hi:[1,0,1]
v_pk_mul_f32 v[116:117], v[116:117], v[174:175] op_sel_hi:[1,0]
v_pk_fma_f32 v[118:119], v[198:199], v[170:171], v[118:119] op_sel_hi:[1,0,1]
v_pk_mul_f32 v[118:119], v[118:119], v[174:175] op_sel_hi:[1,0]
v_cvt_pk_bf16_f32 v112, v112, v113
v_cvt_pk_bf16_f32 v113, v114, v115
v_cvt_pk_bf16_f32 v114, v116, v117
v_cvt_pk_bf16_f32 v115, v118, v119
buffer_store_dwordx4 v[112:115], v161, s[12:15], s64 offen offset:0 sc1
v_pk_fma_f32 v[120:121], v[200:201], v[170:171], v[120:121] op_sel_hi:[1,0,1]
v_pk_mul_f32 v[120:121], v[120:121], v[174:175] op_sel_hi:[1,0]
v_pk_fma_f32 v[122:123], v[202:203], v[170:171], v[122:123] op_sel_hi:[1,0,1]
v_pk_mul_f32 v[122:123], v[122:123], v[174:175] op_sel_hi:[1,0]
v_pk_fma_f32 v[124:125], v[204:205], v[170:171], v[124:125] op_sel_hi:[1,0,1]
v_pk_mul_f32 v[124:125], v[124:125], v[174:175] op_sel_hi:[1,0]
v_pk_fma_f32 v[126:127], v[206:207], v[170:171], v[126:127] op_sel_hi:[1,0,1]
v_pk_mul_f32 v[126:127], v[126:127], v[174:175] op_sel_hi:[1,0]
v_cvt_pk_bf16_f32 v120, v120, v121
v_cvt_pk_bf16_f32 v121, v122, v123
v_cvt_pk_bf16_f32 v122, v124, v125
v_cvt_pk_bf16_f32 v123, v126, v127
buffer_store_dwordx4 v[120:123], v161, s[12:15], s64 offen offset:1024 sc1
s_branch .Lmk_p1epi_11
.Lmk_p1vloop_10:
.Lmk_p1top_15:
s_waitcnt lgkmcnt(0)
v_mfma_f32_32x32x16_bf16 v[64:79], v[128:131], v[136:139], v[64:79]
ds_read_b64_tr_b16 v[152:153], v32 offset:4096
ds_read_b64_tr_b16 v[154:155], v32 offset:5120
ds_read_b128 v[144:147], v25 offset:0
s_lshl_b32 s90, s88, 7
s_mul_i32 s91, s88, 245760
s_add_u32 s88, s88, 1
v_mfma_f32_32x32x16_bf16 v[80:95], v[128:131], v[140:143], v[80:95]
ds_read_b64_tr_b16 v[156:157], v33 offset:4096
ds_read_b64_tr_b16 v[158:159], v33 offset:5120
ds_read_b128 v[148:151], v29 offset:0
s_cmp_eq_u32 s88, 10
s_cselect_b32 s88, 0, s88
s_sub_u32 s89, s89, 1
v_mfma_f32_32x32x16_bf16 v[96:111], v[132:135], v[136:139], v[96:111]
s_cmp_lt_i32 s89, 0
s_cselect_b32 s90, 0x40000000, s90
s_cselect_b32 s91, 0x40000000, s91
s_add_u32 m0, s67, 0
v_mfma_f32_32x32x16_bf16 v[112:127], v[132:135], v[140:143], v[112:127]
buffer_load_dwordx4 v8, s[4:7], s90 offen offset:0 lds
v_mfma_f32_16x16x32_bf16 v[44:47], v[36:39], v[136:139], v[44:47]
buffer_load_dwordx4 v9, s[4:7], s90 offen offset:1024 lds
s_waitcnt lgkmcnt(0)
v_mfma_f32_32x32x16_bf16 v[64:79], v[144:147], v[152:155], v[64:79]
ds_read_b64_tr_b16 v[136:137], v32 offset:8192
ds_read_b64_tr_b16 v[138:139], v32 offset:9216
ds_read_b128 v[128:131], v26 offset:0
v_mfma_f32_32x32x16_bf16 v[80:95], v[144:147], v[156:159], v[80:95]
ds_read_b64_tr_b16 v[140:141], v33 offset:8192
ds_read_b64_tr_b16 v[142:143], v33 offset:9216
ds_read_b128 v[132:135], v30 offset:0
v_mfma_f32_32x32x16_bf16 v[96:111], v[148:151], v[152:155], v[96:111]
v_mfma_f32_32x32x16_bf16 v[112:127], v[148:151], v[156:159], v[112:127]
buffer_load_dwordx4 v10, s[4:7], s90 offen offset:2048 lds
v_mfma_f32_16x16x32_bf16 v[44:47], v[36:39], v[152:155], v[44:47]
buffer_load_dwordx4 v11, s[4:7], s90 offen offset:3072 lds
s_waitcnt lgkmcnt(0)
v_mfma_f32_32x32x16_bf16 v[64:79], v[128:131], v[136:139], v[64:79]
ds_read_b64_tr_b16 v[152:153], v32 offset:12288
ds_read_b64_tr_b16 v[154:155], v32 offset:13312
ds_read_b128 v[144:147], v27 offset:0
v_mfma_f32_32x32x16_bf16 v[80:95], v[128:131], v[140:143], v[80:95]
ds_read_b64_tr_b16 v[156:157], v33 offset:12288
ds_read_b64_tr_b16 v[158:159], v33 offset:13312
ds_read_b128 v[148:151], v31 offset:0
v_mfma_f32_32x32x16_bf16 v[96:111], v[132:135], v[136:139], v[96:111]
s_add_u32 m0, s67, 16384
v_mfma_f32_32x32x16_bf16 v[112:127], v[132:135], v[140:143], v[112:127]
buffer_load_dwordx4 v12, s[8:11], s91 offen offset:0 lds
v_mfma_f32_16x16x32_bf16 v[44:47], v[36:39], v[136:139], v[44:47]
buffer_load_dwordx4 v13, s[8:11], s91 offen offset:1024 lds
s_waitcnt vmcnt(6) lgkmcnt(0)
s_barrier
v_mfma_f32_32x32x16_bf16 v[64:79], v[144:147], v[152:155], v[64:79]
ds_read_b64_tr_b16 v[136:137], v32 offset:32768
ds_read_b64_tr_b16 v[138:139], v32 offset:33792
ds_read_b128 v[128:131], v24 offset:32768
v_mfma_f32_32x32x16_bf16 v[80:95], v[144:147], v[156:159], v[80:95]
ds_read_b64_tr_b16 v[140:141], v33 offset:32768
ds_read_b64_tr_b16 v[142:143], v33 offset:33792
ds_read_b128 v[132:135], v28 offset:32768
v_mfma_f32_32x32x16_bf16 v[96:111], v[148:151], v[152:155], v[96:111]
buffer_load_dwordx4 v[60:63], v59, s[12:15], 0 offen sc1
v_mfma_f32_32x32x16_bf16 v[112:127], v[148:151], v[156:159], v[112:127]
buffer_load_dwordx4 v14, s[8:11], s91 offen offset:2048 lds
v_mfma_f32_16x16x32_bf16 v[44:47], v[36:39], v[152:155], v[44:47]
buffer_load_dwordx4 v15, s[8:11], s91 offen offset:3072 lds
s_waitcnt lgkmcnt(0)
v_mfma_f32_32x32x16_bf16 v[64:79], v[128:131], v[136:139], v[64:79]
ds_read_b64_tr_b16 v[152:153], v32 offset:36864
ds_read_b64_tr_b16 v[154:155], v32 offset:37888
ds_read_b128 v[144:147], v25 offset:32768
s_lshl_b32 s90, s88, 7
s_mul_i32 s91, s88, 245760
s_add_u32 s88, s88, 1
v_mfma_f32_32x32x16_bf16 v[80:95], v[128:131], v[140:143], v[80:95]
ds_read_b64_tr_b16 v[156:157], v33 offset:36864
ds_read_b64_tr_b16 v[158:159], v33 offset:37888
ds_read_b128 v[148:151], v29 offset:32768
s_cmp_eq_u32 s88, 10
s_cselect_b32 s88, 0, s88
s_sub_u32 s89, s89, 1
v_mfma_f32_32x32x16_bf16 v[96:111], v[132:135], v[136:139], v[96:111]
s_cmp_lt_i32 s89, 0
s_cselect_b32 s90, 0x40000000, s90
s_cselect_b32 s91, 0x40000000, s91
s_add_u32 m0, s67, 32768
v_mfma_f32_32x32x16_bf16 v[112:127], v[132:135], v[140:143], v[112:127]
buffer_load_dwordx4 v8, s[4:7], s90 offen offset:0 lds
v_mfma_f32_16x16x32_bf16 v[44:47], v[36:39], v[136:139], v[44:47]
buffer_load_dwordx4 v9, s[4:7], s90 offen offset:1024 lds
s_waitcnt lgkmcnt(0)
v_mfma_f32_32x32x16_bf16 v[64:79], v[144:147], v[152:155], v[64:79]
ds_read_b64_tr_b16 v[136:137], v32 offset:40960
ds_read_b64_tr_b16 v[138:139], v32 offset:41984
ds_read_b128 v[128:131], v26 offset:32768
v_mfma_f32_32x32x16_bf16 v[80:95], v[144:147], v[156:159], v[80:95]
ds_read_b64_tr_b16 v[140:141], v33 offset:40960
ds_read_b64_tr_b16 v[142:143], v33 offset:41984
ds_read_b128 v[132:135], v30 offset:32768
v_mfma_f32_32x32x16_bf16 v[96:111], v[148:151], v[152:155], v[96:111]
v_mfma_f32_32x32x16_bf16 v[112:127], v[148:151], v[156:159], v[112:127]
buffer_load_dwordx4 v10, s[4:7], s90 offen offset:2048 lds
v_mfma_f32_16x16x32_bf16 v[44:47], v[36:39], v[152:155], v[44:47]
buffer_load_dwordx4 v11, s[4:7], s90 offen offset:3072 lds
s_waitcnt lgkmcnt(0)
v_mfma_f32_32x32x16_bf16 v[64:79], v[128:131], v[136:139], v[64:79]
ds_read_b64_tr_b16 v[152:153], v32 offset:45056
ds_read_b64_tr_b16 v[154:155], v32 offset:46080
ds_read_b128 v[144:147], v27 offset:32768
v_mfma_f32_32x32x16_bf16 v[80:95], v[128:131], v[140:143], v[80:95]
ds_read_b64_tr_b16 v[156:157], v33 offset:45056
ds_read_b64_tr_b16 v[158:159], v33 offset:46080
ds_read_b128 v[148:151], v31 offset:32768
v_mfma_f32_32x32x16_bf16 v[96:111], v[132:135], v[136:139], v[96:111]
s_add_u32 m0, s67, 49152
v_mfma_f32_32x32x16_bf16 v[112:127], v[132:135], v[140:143], v[112:127]
buffer_load_dwordx4 v12, s[8:11], s91 offen offset:0 lds
v_mfma_f32_16x16x32_bf16 v[44:47], v[36:39], v[136:139], v[44:47]
buffer_load_dwordx4 v13, s[8:11], s91 offen offset:1024 lds
s_waitcnt vmcnt(6) lgkmcnt(0)
s_barrier
v_xor_b32 v24, 0x10000, v24
v_xor_b32 v28, 0x10000, v28
v_xor_b32 v32, 0x10000, v32
v_xor_b32 v33, 0x10000, v33
v_mfma_f32_32x32x16_bf16 v[64:79], v[144:147], v[152:155], v[64:79]
ds_read_b64_tr_b16 v[136:137], v32 offset:0
ds_read_b64_tr_b16 v[138:139], v32 offset:1024
ds_read_b128 v[128:131], v24 offset:0
v_xor_b32 v25, 0x10000, v25
v_xor_b32 v26, 0x10000, v26
v_xor_b32 v27, 0x10000, v27
v_mfma_f32_32x32x16_bf16 v[80:95], v[144:147], v[156:159], v[80:95]
ds_read_b64_tr_b16 v[140:141], v33 offset:0
ds_read_b64_tr_b16 v[142:143], v33 offset:1024
ds_read_b128 v[132:135], v28 offset:0
v_xor_b32 v29, 0x10000, v29
v_xor_b32 v30, 0x10000, v30
v_xor_b32 v31, 0x10000, v31
v_mfma_f32_32x32x16_bf16 v[96:111], v[148:151], v[152:155], v[96:111]
v_mfma_f32_32x32x16_bf16 v[112:127], v[148:151], v[156:159], v[112:127]
buffer_load_dwordx4 v14, s[8:11], s91 offen offset:2048 lds
v_mfma_f32_16x16x32_bf16 v[44:47], v[36:39], v[152:155], v[44:47]
buffer_load_dwordx4 v15, s[8:11], s91 offen offset:3072 lds
s_xor_b32 s92, s92, 0x10000
s_xor_b32 s67, s67, 0x10000
s_sub_u32 s97, s97, 1
s_cmp_lg_u32 s97, 0
s_cbranch_scc1 .Lmk_p1top_15
s_mul_i32 s59, s47, 5120
s_add_u32 m0, s59, 139264
s_nop 0
buffer_load_dwordx4 v48, s[40:43], 0 offen offset:0 lds
buffer_load_dwordx4 v49, s[40:43], 0 offen offset:1024 lds
buffer_load_dwordx4 v50, s[40:43], 0 offen offset:2048 lds
buffer_load_dwordx4 v51, s[40:43], 0 offen offset:3072 lds
s_add_u32 m0, s59, 143360
s_nop 0
buffer_load_dwordx4 v52, s[40:43], 0 offen lds
.Lmk_xs_16:
v_cmp_ne_u32 vcc, s44, v61
v_cmp_ne_u32 s[50:51], s45, v63
s_or_b64 vcc, vcc, s[50:51]
s_cbranch_vccz .Lmk_xsd_17
buffer_load_dwordx4 v[60:63], v59, s[12:15], 0 offen sc1
s_waitcnt vmcnt(0)
s_sub_u32 s46, s46, 1
s_cbranch_scc0 .Lmk_xs_16
s_mov_b32 s46, 0
.Lmk_xsd_17:
s_lshl_b32 s54, s93, 1
s_add_u32 s54, s54, s94
s_lshl_b32 s54, s54, 7
s_add_u32 s54, s54, 131072
v_lshl_add_u32 v16, v35, 2, s54
s_lshl_b32 s54, s94, 1
s_add_u32 s54, s54, s93
s_lshl_b32 s54, s54, 7
s_add_u32 s54, s54, 131072
v_lshl_add_u32 v17, v35, 2, s54
ds_write_b32 v16, v60
ds_write_b32 v16, v62 offset:512
ds_write_b32 v17, v44 offset:1024
s_waitcnt lgkmcnt(0)
s_barrier
v_xor_b32 v18, 128, v17
ds_read_b32 v168, v17 offset:1024
ds_read_b32 v170, v18 offset:1024
s_lshl_b32 s54, s93, 1
s_xor_b32 s55, s94, 0
s_add_u32 s54, s54, s55
s_lshl_b32 s54, s54, 7
s_add_u32 s54, s54, 131072
v_lshl_add_u32 v19, v34, 4, s54
ds_read_b128 v[176:179], v19 offset:0
ds_read_b128 v[208:211], v19 offset:512
ds_read_b128 v[180:183], v19 offset:32
ds_read_b128 v[212:215], v19 offset:544
ds_read_b128 v[184:187], v19 offset:64
ds_read_b128 v[216:219], v19 offset:576
ds_read_b128 v[188:191], v19 offset:96
ds_read_b128 v[220:223], v19 offset:608
s_waitcnt lgkmcnt(0)
s_lshl_b32 s54, s93, 1
s_xor_b32 s55, s94, 1
s_add_u32 s54, s54, s55
s_lshl_b32 s54, s54, 7
s_add_u32 s54, s54, 131072
v_lshl_add_u32 v19, v34, 4, s54
ds_read_b128 v[192:195], v19 offset:0
ds_read_b128 v[224:227], v19 offset:512
ds_read_b128 v[196:199], v19 offset:32
ds_read_b128 v[228:231], v19 offset:544
ds_read_b128 v[200:203], v19 offset:64
ds_read_b128 v[232:235], v19 offset:576
ds_read_b128 v[204:207], v19 offset:96
ds_read_b128 v[236:239], v19 offset:608
s_waitcnt lgkmcnt(0)
v_pk_fma_f32 v[64:65], v[176:177], v[168:169], v[64:65] op_sel_hi:[1,0,1]
v_pk_mul_f32 v[64:65], v[64:65], v[208:209]
v_pk_fma_f32 v[66:67], v[178:179], v[168:169], v[66:67] op_sel_hi:[1,0,1]
v_pk_mul_f32 v[66:67], v[66:67], v[210:211]
v_pk_fma_f32 v[68:69], v[180:181], v[168:169], v[68:69] op_sel_hi:[1,0,1]
v_pk_mul_f32 v[68:69], v[68:69], v[212:213]
v_pk_fma_f32 v[70:71], v[182:183], v[168:169], v[70:71] op_sel_hi:[1,0,1]
)ASM"
      R"ASM(
v_pk_mul_f32 v[70:71], v[70:71], v[214:215]
v_cvt_pk_bf16_f32 v64, v64, v65
v_cvt_pk_bf16_f32 v65, v66, v67
v_cvt_pk_bf16_f32 v66, v68, v69
v_cvt_pk_bf16_f32 v67, v70, v71
buffer_store_dwordx4 v[64:67], v160, s[12:15], s63 offen sc1
v_pk_fma_f32 v[72:73], v[184:185], v[168:169], v[72:73] op_sel_hi:[1,0,1]
v_pk_mul_f32 v[72:73], v[72:73], v[216:217]
v_pk_fma_f32 v[74:75], v[186:187], v[168:169], v[74:75] op_sel_hi:[1,0,1]
v_pk_mul_f32 v[74:75], v[74:75], v[218:219]
v_pk_fma_f32 v[76:77], v[188:189], v[168:169], v[76:77] op_sel_hi:[1,0,1]
v_pk_mul_f32 v[76:77], v[76:77], v[220:221]
v_pk_fma_f32 v[78:79], v[190:191], v[168:169], v[78:79] op_sel_hi:[1,0,1]
v_pk_mul_f32 v[78:79], v[78:79], v[222:223]
v_cvt_pk_bf16_f32 v72, v72, v73
v_cvt_pk_bf16_f32 v73, v74, v75
v_cvt_pk_bf16_f32 v74, v76, v77
v_cvt_pk_bf16_f32 v75, v78, v79
buffer_store_dwordx4 v[72:75], v162, s[12:15], s63 offen sc1
v_pk_fma_f32 v[80:81], v[176:177], v[170:171], v[80:81] op_sel_hi:[1,0,1]
v_pk_mul_f32 v[80:81], v[80:81], v[208:209]
v_pk_fma_f32 v[82:83], v[178:179], v[170:171], v[82:83] op_sel_hi:[1,0,1]
v_pk_mul_f32 v[82:83], v[82:83], v[210:211]
v_pk_fma_f32 v[84:85], v[180:181], v[170:171], v[84:85] op_sel_hi:[1,0,1]
v_pk_mul_f32 v[84:85], v[84:85], v[212:213]
v_pk_fma_f32 v[86:87], v[182:183], v[170:171], v[86:87] op_sel_hi:[1,0,1]
v_pk_mul_f32 v[86:87], v[86:87], v[214:215]
v_cvt_pk_bf16_f32 v80, v80, v81
v_cvt_pk_bf16_f32 v81, v82, v83
v_cvt_pk_bf16_f32 v82, v84, v85
v_cvt_pk_bf16_f32 v83, v86, v87
buffer_store_dwordx4 v[80:83], v161, s[12:15], s63 offen sc1
v_pk_fma_f32 v[88:89], v[184:185], v[170:171], v[88:89] op_sel_hi:[1,0,1]
v_pk_mul_f32 v[88:89], v[88:89], v[216:217]
v_pk_fma_f32 v[90:91], v[186:187], v[170:171], v[90:91] op_sel_hi:[1,0,1]
v_pk_mul_f32 v[90:91], v[90:91], v[218:219]
v_pk_fma_f32 v[92:93], v[188:189], v[170:171], v[92:93] op_sel_hi:[1,0,1]
v_pk_mul_f32 v[92:93], v[92:93], v[220:221]
v_pk_fma_f32 v[94:95], v[190:191], v[170:171], v[94:95] op_sel_hi:[1,0,1]
v_pk_mul_f32 v[94:95], v[94:95], v[222:223]
v_cvt_pk_bf16_f32 v88, v88, v89
v_cvt_pk_bf16_f32 v89, v90, v91
v_cvt_pk_bf16_f32 v90, v92, v93
v_cvt_pk_bf16_f32 v91, v94, v95
buffer_store_dwordx4 v[88:91], v163, s[12:15], s63 offen sc1
v_pk_fma_f32 v[96:97], v[192:193], v[168:169], v[96:97] op_sel_hi:[1,0,1]
v_pk_mul_f32 v[96:97], v[96:97], v[224:225]
v_pk_fma_f32 v[98:99], v[194:195], v[168:169], v[98:99] op_sel_hi:[1,0,1]
v_pk_mul_f32 v[98:99], v[98:99], v[226:227]
v_pk_fma_f32 v[100:101], v[196:197], v[168:169], v[100:101] op_sel_hi:[1,0,1]
v_pk_mul_f32 v[100:101], v[100:101], v[228:229]
v_pk_fma_f32 v[102:103], v[198:199], v[168:169], v[102:103] op_sel_hi:[1,0,1]
v_pk_mul_f32 v[102:103], v[102:103], v[230:231]
v_cvt_pk_bf16_f32 v96, v96, v97
v_cvt_pk_bf16_f32 v97, v98, v99
v_cvt_pk_bf16_f32 v98, v100, v101
v_cvt_pk_bf16_f32 v99, v102, v103
buffer_store_dwordx4 v[96:99], v160, s[12:15], s64 offen sc1
v_pk_fma_f32 v[104:105], v[200:201], v[168:169], v[104:105] op_sel_hi:[1,0,1]
v_pk_mul_f32 v[104:105], v[104:105], v[232:233]
v_pk_fma_f32 v[106:107], v[202:203], v[168:169], v[106:107] op_sel_hi:[1,0,1]
v_pk_mul_f32 v[106:107], v[106:107], v[234:235]
v_pk_fma_f32 v[108:109], v[204:205], v[168:169], v[108:109] op_sel_hi:[1,0,1]
v_pk_mul_f32 v[108:109], v[108:109], v[236:237]
v_pk_fma_f32 v[110:111], v[206:207], v[168:169], v[110:111] op_sel_hi:[1,0,1]
v_pk_mul_f32 v[110:111], v[110:111], v[238:239]
v_cvt_pk_bf16_f32 v104, v104, v105
v_cvt_pk_bf16_f32 v105, v106, v107
v_cvt_pk_bf16_f32 v106, v108, v109
v_cvt_pk_bf16_f32 v107, v110, v111
buffer_store_dwordx4 v[104:107], v162, s[12:15], s64 offen sc1
v_pk_fma_f32 v[112:113], v[192:193], v[170:171], v[112:113] op_sel_hi:[1,0,1]
v_pk_mul_f32 v[112:113], v[112:113], v[224:225]
v_pk_fma_f32 v[114:115], v[194:195], v[170:171], v[114:115] op_sel_hi:[1,0,1]
v_pk_mul_f32 v[114:115], v[114:115], v[226:227]
v_pk_fma_f32 v[116:117], v[196:197], v[170:171], v[116:117] op_sel_hi:[1,0,1]
v_pk_mul_f32 v[116:117], v[116:117], v[228:229]
v_pk_fma_f32 v[118:119], v[198:199], v[170:171], v[118:119] op_sel_hi:[1,0,1]
v_pk_mul_f32 v[118:119], v[118:119], v[230:231]
v_cvt_pk_bf16_f32 v112, v112, v113
v_cvt_pk_bf16_f32 v113, v114, v115
v_cvt_pk_bf16_f32 v114, v116, v117
v_cvt_pk_bf16_f32 v115, v118, v119
buffer_store_dwordx4 v[112:115], v161, s[12:15], s64 offen sc1
v_pk_fma_f32 v[120:121], v[200:201], v[170:171], v[120:121] op_sel_hi:[1,0,1]
v_pk_mul_f32 v[120:121], v[120:121], v[232:233]
v_pk_fma_f32 v[122:123], v[202:203], v[170:171], v[122:123] op_sel_hi:[1,0,1]
v_pk_mul_f32 v[122:123], v[122:123], v[234:235]
v_pk_fma_f32 v[124:125], v[204:205], v[170:171], v[124:125] op_sel_hi:[1,0,1]
v_pk_mul_f32 v[124:125], v[124:125], v[236:237]
v_pk_fma_f32 v[126:127], v[206:207], v[170:171], v[126:127] op_sel_hi:[1,0,1]
v_pk_mul_f32 v[126:127], v[126:127], v[238:239]
v_cvt_pk_bf16_f32 v120, v120, v121
v_cvt_pk_bf16_f32 v121, v122, v123
v_cvt_pk_bf16_f32 v122, v124, v125
v_cvt_pk_bf16_f32 v123, v126, v127
buffer_store_dwordx4 v[120:123], v163, s[12:15], s64 offen sc1
.Lmk_p1epi_11:
s_waitcnt vmcnt(0)
v_mov_b32 v4, s44
v_mov_b32 v5, s45
s_mov_b64 exec, 0xffff
buffer_store_dwordx2 v[4:5], v3, s[12:15], s65 offen sc1
s_mov_b64 exec, -1
s_branch .Lmk_p1join_18
.Lmk_p1skip_2:
s_lshl_b32 s50, s2, 2
s_add_u32 s50, s50, s47
s_mul_i32 s59, s47, 5120
s_add_u32 s59, s59, 139264
s_cmp_ge_u32 s50, 592
s_cbranch_scc1 .Lmk_wt1_19
s_lshl_b32 s72, s50, 7
s_add_u32 s72, s72, 131072
s_cmp_ge_u32 s50, 160
s_cbranch_scc1 .Lmk_wt2_20
s_mov_b32 s40, s20
s_mov_b32 s41, s21
s_mov_b32 s42, s22
s_mov_b32 s43, s23
s_mul_i32 s71, s50, 5120
s_add_u32 s71, s71, 24117248
s_mul_i32 s55, s50, 3277
s_lshr_b32 s55, s55, 16
s_mul_i32 s57, s55, 20
s_sub_u32 s57, s50, s57
s_mul_i32 s58, s57, 205
s_lshr_b32 s58, s58, 10
s_lshr_b32 s56, s58, 1
s_mul_i32 s51, s58, 5
s_sub_u32 s57, s57, s51
s_mul_i32 s51, s58, 160
s_mul_i32 s58, s57, 40
s_cmp_eq_u32 s57, 4
s_cselect_b32 s58, 16, s58
s_mov_b32 s52, 20
s_cselect_b32 s52, 80, s52
s_cselect_b32 s53, 40, 4
s_cselect_b32 s54, 20, 8
s_add_u32 s51, s51, s58
s_branch .Lmk_wt3_21
.Lmk_wt2_20:
s_mov_b32 s40, s28
s_mov_b32 s41, s29
s_mov_b32 s42, s30
s_mov_b32 s43, s31
s_sub_u32 s57, s50, 160
s_mul_i32 s71, s57, 5120
s_add_u32 s71, s71, 25165824
s_mul_i32 s55, s57, 1214
s_lshr_b32 s55, s55, 16
s_mul_i32 s58, s55, 54
s_sub_u32 s57, s57, s58
s_lshl_b32 s51, s57, 5
s_cmp_ge_u32 s57, 28
s_cselect_b32 s56, 1, 0
s_mov_b32 s52, 16
s_mov_b32 s53, 4
s_mov_b32 s54, 8
.Lmk_wt3_21:
s_mul_i32 s55, s55, 160
s_mul_i32 s51, s51, 1280
s_add_u32 s51, s51, s55
v_add_u32 v16, 0, v0
v_mul_u32_u24 v17, 6554, v16
v_lshrrev_b32 v17, 16, v17
v_mul_u32_u24 v18, 10, v17
v_sub_u32 v16, v16, v18
v_and_b32 v18, 3, v17
v_bfe_u32 v19, v17, 2, 1
v_mad_u32_u24 v18, v19, s54, v18
v_bfe_u32 v19, v17, 3, 1
v_mad_u32_u24 v18, v19, s53, v18
v_lshrrev_b32 v19, 4, v17
v_mad_u32_u24 v18, v19, s52, v18
v_mul_u32_u24 v18, 1280, v18
v_lshl_add_u32 v18, v16, 4, v18
s_sub_u32 s57, s51, 0
v_add_u32 v48, s57, v18
v_add_u32 v16, 64, v0
v_mul_u32_u24 v17, 6554, v16
v_lshrrev_b32 v17, 16, v17
v_mul_u32_u24 v18, 10, v17
v_sub_u32 v16, v16, v18
v_and_b32 v18, 3, v17
v_bfe_u32 v19, v17, 2, 1
v_mad_u32_u24 v18, v19, s54, v18
v_bfe_u32 v19, v17, 3, 1
v_mad_u32_u24 v18, v19, s53, v18
v_lshrrev_b32 v19, 4, v17
v_mad_u32_u24 v18, v19, s52, v18
v_mul_u32_u24 v18, 1280, v18
v_lshl_add_u32 v18, v16, 4, v18
s_sub_u32 s57, s51, 1024
v_add_u32 v49, s57, v18
v_add_u32 v16, 128, v0
v_mul_u32_u24 v17, 6554, v16
v_lshrrev_b32 v17, 16, v17
v_mul_u32_u24 v18, 10, v17
v_sub_u32 v16, v16, v18
v_and_b32 v18, 3, v17
v_bfe_u32 v19, v17, 2, 1
v_mad_u32_u24 v18, v19, s54, v18
v_bfe_u32 v19, v17, 3, 1
v_mad_u32_u24 v18, v19, s53, v18
v_lshrrev_b32 v19, 4, v17
v_mad_u32_u24 v18, v19, s52, v18
v_mul_u32_u24 v18, 1280, v18
v_lshl_add_u32 v18, v16, 4, v18
s_sub_u32 s57, s51, 2048
v_add_u32 v50, s57, v18
v_add_u32 v16, 192, v0
v_mul_u32_u24 v17, 6554, v16
v_lshrrev_b32 v17, 16, v17
v_mul_u32_u24 v18, 10, v17
v_sub_u32 v16, v16, v18
v_and_b32 v18, 3, v17
v_bfe_u32 v19, v17, 2, 1
v_mad_u32_u24 v18, v19, s54, v18
v_bfe_u32 v19, v17, 3, 1
v_mad_u32_u24 v18, v19, s53, v18
v_lshrrev_b32 v19, 4, v17
v_mad_u32_u24 v18, v19, s52, v18
v_mul_u32_u24 v18, 1280, v18
v_lshl_add_u32 v18, v16, 4, v18
s_sub_u32 s57, s51, 3072
v_add_u32 v51, s57, v18
v_add_u32 v16, 256, v0
v_mul_u32_u24 v17, 6554, v16
v_lshrrev_b32 v17, 16, v17
v_mul_u32_u24 v18, 10, v17
v_sub_u32 v16, v16, v18
v_and_b32 v18, 3, v17
v_bfe_u32 v19, v17, 2, 1
v_mad_u32_u24 v18, v19, s54, v18
v_bfe_u32 v19, v17, 3, 1
v_mad_u32_u24 v18, v19, s53, v18
v_lshrrev_b32 v19, 4, v17
v_mad_u32_u24 v18, v19, s52, v18
v_mul_u32_u24 v18, 1280, v18
v_lshl_add_u32 v18, v16, 4, v18
s_sub_u32 s57, s51, 0
v_add_u32 v52, s57, v18
v_lshrrev_b32 v16, 4, v0
v_bfe_u32 v17, v0, 2, 2
v_lshl_add_u32 v16, v16, 3, v17
v_mul_u32_u24 v16, 160, v16
v_add_u32 v16, s59, v16
v_and_b32 v17, 3, v0
v_lshl_add_u32 v18, v17, 4, v16
s_lshl_b32 s56, s56, 6
s_xor_b32 s57, s56, 64
v_add_u32 v53, s56, v18
v_add_u32 v55, 8, v53
v_add_u32 v57, s57, v18
v_add_u32 v40, 8, v57
v_lshl_add_u32 v42, v17, 3, v16
v_add_u32 v42, 128, v42
v_add_u32 v54, 640, v53
v_add_u32 v56, 640, v55
v_add_u32 v58, 640, v57
v_add_u32 v41, 640, v40
v_add_u32 v43, 640, v42
s_branch .Lmk_wtd_22
.Lmk_wt1_19:
s_mov_b32 s40, s24
s_mov_b32 s41, s25
s_mov_b32 s42, s26
s_mov_b32 s43, s27
s_sub_u32 s51, s50, 592
s_lshr_b32 s52, s51, 4
s_lshl_b32 s52, s52, 1
s_and_b32 s53, s51, 1
s_add_u32 s52, s52, s53
s_bfe_u32 s53, s51, 0x30001
s_lshl_b32 s54, s52, 3
s_add_u32 s54, s54, s53
s_mul_i32 s71, s54, 5120
s_add_u32 s71, s71, 28311552
s_add_u32 s54, s54, 592
s_lshl_b32 s72, s54, 7
s_add_u32 s72, s72, 131072
s_mul_i32 s53, s53, 276480
s_lshl_b32 s52, s52, 6
s_add_u32 s53, s53, s52
v_lshrrev_b32 v16, 2, v0
v_mul_u32_u24 v16, 3456, v16
v_and_b32 v17, 3, v0
v_lshl_add_u32 v16, v17, 4, v16
v_add_u32 v48, s53, v16
v_add_u32 v49, 54272, v48
v_add_u32 v50, 108544, v48
v_add_u32 v51, 162816, v48
v_add_u32 v52, 221184, v48
v_lshrrev_b32 v16, 5, v0
v_bfe_u32 v18, v0, 2, 2
v_lshl_add_u32 v16, v16, 3, v18
v_lshlrev_b32 v16, 6, v16
v_bfe_u32 v18, v0, 4, 1
v_lshl_add_u32 v16, v18, 5, v16
v_lshl_add_u32 v16, v17, 3, v16
v_add_u32 v53, s59, v16
v_add_u32 v54, 256, v53
v_add_u32 v55, 1024, v53
v_add_u32 v56, 1280, v53
v_add_u32 v57, 2048, v53
v_add_u32 v58, 2304, v53
v_add_u32 v40, 3072, v53
v_add_u32 v41, 3328, v53
v_add_u32 v42, 4096, v53
v_add_u32 v43, 4352, v53
.Lmk_wtd_22:
s_mul_i32 s59, s47, 5120
s_add_u32 m0, s59, 139264
s_nop 0
buffer_load_dwordx4 v48, s[40:43], 0 offen offset:0 lds
buffer_load_dwordx4 v49, s[40:43], 0 offen offset:1024 lds
buffer_load_dwordx4 v50, s[40:43], 0 offen offset:2048 lds
buffer_load_dwordx4 v51, s[40:43], 0 offen offset:3072 lds
s_add_u32 m0, s59, 143360
s_nop 0
buffer_load_dwordx4 v52, s[40:43], 0 offen lds
s_waitcnt vmcnt(0)
v_readfirstlane_b32 s44, v6
v_readfirstlane_b32 s45, v7
s_add_u32 s44, s44, 1
s_addc_u32 s45, s45, 0
v_mov_b32 v236, 0
v_mov_b32 v237, 0
v_mov_b32 v238, 0x3f803f80
v_dot2c_f32_bf16 v236, v244, v238
v_dot2c_f32_bf16 v237, v244, v244
v_dot2c_f32_bf16 v236, v245, v238
v_dot2c_f32_bf16 v237, v245, v245
v_dot2c_f32_bf16 v236, v246, v238
v_dot2c_f32_bf16 v237, v246, v246
v_dot2c_f32_bf16 v236, v247, v238
v_dot2c_f32_bf16 v237, v247, v247
v_dot2c_f32_bf16 v236, v248, v238
v_dot2c_f32_bf16 v237, v248, v248
v_dot2c_f32_bf16 v236, v249, v238
v_dot2c_f32_bf16 v237, v249, v249
v_dot2c_f32_bf16 v236, v250, v238
v_dot2c_f32_bf16 v237, v250, v250
v_dot2c_f32_bf16 v236, v251, v238
v_dot2c_f32_bf16 v237, v251, v251
v_dot2c_f32_bf16 v236, v252, v238
v_dot2c_f32_bf16 v237, v252, v252
v_dot2c_f32_bf16 v236, v253, v238
v_dot2c_f32_bf16 v237, v253, v253
s_nop 1
s_nop 0
v_add_f32_dpp v236, v236, v236 row_ror:8 row_mask:0xf bank_mask:0xf
v_add_f32_dpp v237, v237, v237 row_ror:8 row_mask:0xf bank_mask:0xf
s_nop 0
v_add_f32_dpp v236, v236, v236 row_ror:4 row_mask:0xf bank_mask:0xf
v_add_f32_dpp v237, v237, v237 row_ror:4 row_mask:0xf bank_mask:0xf
s_nop 0
v_add_f32_dpp v236, v236, v236 row_ror:2 row_mask:0xf bank_mask:0xf
v_add_f32_dpp v237, v237, v237 row_ror:2 row_mask:0xf bank_mask:0xf
s_nop 0
v_add_f32_dpp v236, v236, v236 row_ror:1 row_mask:0xf bank_mask:0xf
v_add_f32_dpp v237, v237, v237 row_ror:1 row_mask:0xf bank_mask:0xf
v_mov_b32 v240, v236
v_mov_b32 v241, v237
s_nop 0
v_permlane16_swap_b32 v236, v240
v_permlane16_swap_b32 v237, v241
v_add_f32 v236, v236, v240
v_add_f32 v237, v237, v241
v_mul_f32 v240, 0xbacccccd, v236
v_mul_f32 v237, 0x3acccccd, v237
v_fma_f32 v237, -v240, v240, v237
v_max_f32 v237, 0, v237
v_add_f32 v237, 0x3727c5ac, v237
v_rsq_f32 v242, v237
v_mov_b32 v241, s44
v_mov_b32 v243, s45
s_lshl_b32 s50, s70, 4
s_add_u32 s50, s50, 1507328
v_lshrrev_b32 v236, 1, v0
s_mov_b32 exec_lo, 1
s_mov_b32 exec_hi, 1
buffer_store_dwordx4 v[240:243], v236, s[12:15], s50 offen sc1
s_mov_b64 exec, -1
s_cmp_lg_u32 s2, 225
s_cbranch_scc1 .Lmk_p1join_18
v_mov_b32 v8, 0x3f803f80
v_mov_b32 v9, 0x3f803f80
v_mov_b32 v10, 0x3f803f80
v_mov_b32 v11, 0x3f803f80
v_lshrrev_b32 v12, 3, v0
v_mul_u32_u24 v12, 82048, v12
v_and_b32 v13, 7, v0
v_lshl_add_u32 v12, v13, 4, v12
s_mul_i32 s50, s47, 1312768
s_add_u32 s50, s50, 4800512
s_add_u32 s51, s50, 656384
buffer_store_dwordx4 v[8:11], v12, s[12:15], s50 offen sc1
buffer_store_dwordx4 v[8:11], v12, s[12:15], s51 offen sc1
)ASM"
      R"ASM(
s_lshl_b32 s50, s47, 7
s_add_u32 s50, s50, 126976
s_waitcnt vmcnt(0)
v_mov_b32 v4, s44
v_mov_b32 v5, s45
s_mov_b64 exec, 0xffff
buffer_store_dwordx2 v[4:5], v3, s[12:15], s50 offen sc1
s_mov_b64 exec, -1
.Lmk_p1join_18:
ds_read_b64_tr_b16 v[64:65], v53
ds_read_b64_tr_b16 v[66:67], v54
ds_read_b64_tr_b16 v[68:69], v55
ds_read_b64_tr_b16 v[70:71], v56
ds_read_b64_tr_b16 v[72:73], v57
ds_read_b64_tr_b16 v[74:75], v58
ds_read_b64_tr_b16 v[76:77], v40
ds_read_b64_tr_b16 v[78:79], v41
ds_read_b64_tr_b16 v[80:81], v42
ds_read_b64_tr_b16 v[82:83], v43
s_add_u32 s50, s71, 4096
s_waitcnt lgkmcnt(8)
buffer_store_dwordx4 v[64:67], v2, s[12:15], s71 offen offset:0 sc1
s_waitcnt lgkmcnt(6)
buffer_store_dwordx4 v[68:71], v2, s[12:15], s71 offen offset:1024 sc1
s_waitcnt lgkmcnt(4)
buffer_store_dwordx4 v[72:75], v2, s[12:15], s71 offen offset:2048 sc1
s_waitcnt lgkmcnt(2)
buffer_store_dwordx4 v[76:79], v2, s[12:15], s71 offen offset:3072 sc1
s_waitcnt lgkmcnt(0)
buffer_store_dwordx4 v[80:83], v2, s[12:15], s50 offen sc1
s_lshr_b32 s50, s49, 3
s_lshl_b32 s86, s48, 2
s_add_u32 s86, s86, s50
s_and_b32 s87, s49, 7
s_lshr_b32 s91, s49, 4
s_bfe_u32 s90, s49, 0x10003
s_and_b32 s89, s49, 7
s_and_b32 s50, s49, 15
s_lshl_b32 s92, s50, 2
s_add_u32 s92, s92, s47
s_mul_i32 s93, s47, 14
s_cmp_eq_u32 s47, 3
s_cselect_b32 s93, 41, s93
v_lshrrev_b32 v16, 5, v0
v_and_b32 v17, 31, v0
v_bfe_u32 v18, v0, 1, 3
s_lshl_b32 s50, s47, 3
v_add_u32 v18, s50, v18
v_bfe_u32 v19, v0, 1, 2
s_lshl_b32 s50, s87, 2
v_add_u32 v19, s50, v19
v_cmp_gt_u32 vcc, 32, v0
v_and_b32 v20, 1, v0
v_mul_u32_u24 v20, 19, v20
v_cndmask_b32 v18, v19, v18, vcc
v_lshrrev_b32 v19, 4, v0
v_add_u32 v19, 1, v19
v_mul_u32_u24 v19, 640, v19
v_cndmask_b32 v19, 0, v19, vcc
s_mul_i32 s50, s86, 20
v_add3_u32 v19, v19, v20, s50
v_lshrrev_b32 v19, 6, v19
v_mad_u32_u24 v18, v18, 30, v19
s_add_u32 s50, s47, 960
v_mov_b32 v19, s50
v_cmp_gt_u32 vcc, 40, v0
s_nop 1
v_cndmask_b32 v18, v19, v18, vcc
v_lshlrev_b32 v4, 7, v18
s_lshr_b32 s50, s86, 1
s_mul_i32 s50, s50, 2560
s_and_b32 s51, s86, 1
s_mul_i32 s52, s51, 1536
s_add_u32 s52, s52, s50
v_lshl_add_u32 v18, v17, 4, s52
v_lshl_add_u32 v8, v16, 9, v18
s_lshl_b32 s51, s51, 3
s_add_u32 s51, s51, s50
s_add_u32 s51, s51, 1024
v_lshl_add_u32 v9, v17, 4, s51
s_mul_i32 s50, s86, 320
s_add_u32 s50, s50, 40960
v_lshl_add_u32 v18, v17, 4, s50
v_mul_u32_u24 v19, 10240, v16
v_add_u32 v18, v18, v19
v_add_u32 v19, 20480, v18
v_cmp_gt_u32 vcc, 20, v17
v_mov_b32 v20, 81920
s_nop 0
v_cndmask_b32 v10, v20, v18, vcc
v_cndmask_b32 v11, v20, v19, vcc
s_mul_i32 s88, s87, 327680
s_add_u32 s88, s88, 2097152
s_add_u32 s50, s47, 1
s_and_b32 s50, s50, 3
s_mul_i32 s50, s50, 40960
s_add_u32 s60, s50, s88
s_add_u32 s50, s47, 2
s_and_b32 s50, s50, 3
s_mul_i32 s50, s50, 40960
s_add_u32 s61, s50, s88
s_add_u32 s50, s47, 3
s_and_b32 s50, s50, 3
s_mul_i32 s50, s50, 40960
s_add_u32 s62, s50, s88
s_add_u32 s50, s47, 4
s_and_b32 s50, s50, 3
s_mul_i32 s50, s50, 40960
s_add_u32 s63, s50, s88
s_add_u32 s50, s47, 1
v_add_u32 v18, s50, v16
v_and_b32 v18, 3, v18
v_mul_u32_u24 v18, 40960, v18
v_add_u32 v12, v18, v9
s_add_u32 s50, s47, 3
v_add_u32 v18, s50, v16
v_and_b32 v18, 3, v18
v_mul_u32_u24 v18, 40960, v18
v_add_u32 v13, v18, v9
v_mov_b32 v14, 0xdddd
s_lshl_b32 s50, s87, 3
s_add_u32 s50, s50, s47
s_lshl_b32 s50, s50, 1
v_bfe_u32 v18, v0, 4, 1
v_add_u32 v18, s50, v18
v_and_b32 v19, 15, v0
s_lshl_b32 s51, s86, 9
s_add_u32 s51, s51, 10485760
v_lshl_add_u32 v20, v18, 14, s51
v_lshl_add_u32 v20, v16, 8, v20
v_lshl_add_u32 v214, v19, 4, v20
s_lshl_b32 s51, s86, 7
s_add_u32 s51, s51, 12582912
v_lshl_add_u32 v20, v18, 12, s51
v_lshl_add_u32 v20, v19, 3, v20
v_lshl_add_u32 v215, v16, 30, v20
s_lshl_b32 s50, s87, 2
s_add_u32 s50, s50, s47
s_lshl_b32 s50, s50, 5
s_add_u32 s50, s50, s86
s_lshl_b32 s50, s50, 7
s_add_u32 s68, s50, 262144
s_mul_i32 s64, s92, 40960
s_add_u32 s64, s64, 28311552
s_cmp_ge_u32 s92, 54
s_cselect_b32 s64, 0x40000000, s64
s_mul_i32 s50, s89, 54
s_add_u32 s50, s50, s93
s_mul_i32 s65, s50, 5120
s_add_u32 s65, s65, 25165824
s_mul_i32 s67, s47, 25600
s_mul_i32 s66, s89, 102400
s_add_u32 s66, s66, s67
s_add_u32 s66, s66, 24117248
s_mov_b32 s50, 4096
.Lmk_poll_23:
buffer_load_dwordx2 v[6:7], v4, s[12:15], s50 offen sc1
s_waitcnt vmcnt(0)
v_cmp_ne_u64 vcc, s[44:45], v[6:7]
s_nop 0
s_cbranch_vccz .Lmk_polld_24
s_sub_u32 s46, s46, 1
s_cbranch_scc0 .Lmk_poll_23
s_mov_b32 s46, 0
.Lmk_polld_24:
v_mov_b32 v4, s44
v_mov_b32 v5, s45
s_mov_b64 exec, 0xffff
buffer_store_dwordx2 v[4:5], v3, s[12:15], s72 offen sc1
s_mov_b64 exec, -1
s_lshl_b32 s50, s87, 1
s_add_u32 s50, s50, 0
s_and_b32 s50, s50, 15
s_lshl_b32 s51, s47, 4
s_add_u32 s50, s50, s51
s_mul_i32 s50, s50, 82048
s_add_u32 s70, s50, 4718592
buffer_load_dwordx4 v[144:147], v8, s[12:15], s60 offen sc1
buffer_load_dwordx4 v[148:151], v8, s[12:15], s61 offen sc1
buffer_load_dwordx2 v[160:161], v12, s[12:15], s88 offen sc1
buffer_load_dwordx4 v[164:167], v8, s[12:15], s70 offen sc1
buffer_load_dwordx2 v[168:169], v9, s[12:15], s70 offen sc1
buffer_load_dwordx4 v[152:155], v8, s[12:15], s62 offen sc1
buffer_load_dwordx4 v[156:159], v8, s[12:15], s63 offen sc1
buffer_load_dwordx2 v[162:163], v13, s[12:15], s88 offen sc1
buffer_load_dwordx4 v[182:185], v10, s[12:15], s70 offen sc1
buffer_load_dwordx4 v[186:189], v11, s[12:15], s70 offen sc1
s_lshl_b32 s50, s87, 1
s_add_u32 s50, s50, 1
s_and_b32 s50, s50, 15
s_lshl_b32 s51, s47, 4
s_add_u32 s50, s50, s51
s_mul_i32 s50, s50, 82048
s_add_u32 s71, s50, 4718592
buffer_load_dwordx4 v[170:173], v8, s[12:15], s71 offen sc1
buffer_load_dwordx2 v[174:175], v9, s[12:15], s71 offen sc1
buffer_load_dwordx4 v[190:193], v10, s[12:15], s71 offen sc1
buffer_load_dwordx4 v[194:197], v11, s[12:15], s71 offen sc1
s_lshl_b32 s50, s87, 1
s_add_u32 s50, s50, 2
s_and_b32 s50, s50, 15
s_lshl_b32 s51, s47, 4
s_add_u32 s50, s50, s51
s_mul_i32 s50, s50, 82048
s_add_u32 s72, s50, 4718592
buffer_load_dwordx4 v[176:179], v8, s[12:15], s72 offen sc1
buffer_load_dwordx2 v[180:181], v9, s[12:15], s72 offen sc1
buffer_load_dwordx4 v[198:201], v10, s[12:15], s72 offen sc1
buffer_load_dwordx4 v[202:205], v11, s[12:15], s72 offen sc1
s_lshl_b32 s50, s87, 1
s_add_u32 s50, s50, 3
s_and_b32 s50, s50, 15
s_lshl_b32 s51, s47, 4
s_add_u32 s50, s50, s51
s_mul_i32 s50, s50, 82048
s_add_u32 s73, s50, 4718592
s_lshl_b32 s50, s87, 1
s_add_u32 s50, s50, 4
s_and_b32 s50, s50, 15
s_lshl_b32 s51, s47, 4
s_add_u32 s50, s50, s51
s_mul_i32 s50, s50, 82048
s_add_u32 s74, s50, 4718592
s_lshl_b32 s50, s87, 1
s_add_u32 s50, s50, 5
s_and_b32 s50, s50, 15
s_lshl_b32 s51, s47, 4
s_add_u32 s50, s50, s51
s_mul_i32 s50, s50, 82048
s_add_u32 s75, s50, 4718592
s_lshl_b32 s50, s87, 1
s_add_u32 s50, s50, 6
s_and_b32 s50, s50, 15
s_lshl_b32 s51, s47, 4
s_add_u32 s50, s50, s51
s_mul_i32 s50, s50, 82048
s_add_u32 s76, s50, 4718592
s_lshl_b32 s50, s87, 1
s_add_u32 s50, s50, 7
s_and_b32 s50, s50, 15
s_lshl_b32 s51, s47, 4
s_add_u32 s50, s50, s51
s_mul_i32 s50, s50, 82048
s_add_u32 s77, s50, 4718592
s_lshl_b32 s50, s87, 1
s_add_u32 s50, s50, 8
s_and_b32 s50, s50, 15
s_lshl_b32 s51, s47, 4
s_add_u32 s50, s50, s51
s_mul_i32 s50, s50, 82048
s_add_u32 s78, s50, 4718592
s_lshl_b32 s50, s87, 1
s_add_u32 s50, s50, 9
s_and_b32 s50, s50, 15
s_lshl_b32 s51, s47, 4
s_add_u32 s50, s50, s51
s_mul_i32 s50, s50, 82048
s_add_u32 s79, s50, 4718592
s_lshl_b32 s50, s87, 1
s_add_u32 s50, s50, 10
s_and_b32 s50, s50, 15
s_lshl_b32 s51, s47, 4
s_add_u32 s50, s50, s51
s_mul_i32 s50, s50, 82048
s_add_u32 s80, s50, 4718592
s_lshl_b32 s50, s87, 1
s_add_u32 s50, s50, 11
s_and_b32 s50, s50, 15
s_lshl_b32 s51, s47, 4
s_add_u32 s50, s50, s51
s_mul_i32 s50, s50, 82048
s_add_u32 s81, s50, 4718592
s_lshl_b32 s50, s87, 1
s_add_u32 s50, s50, 12
s_and_b32 s50, s50, 15
s_lshl_b32 s51, s47, 4
s_add_u32 s50, s50, s51
s_mul_i32 s50, s50, 82048
s_add_u32 s82, s50, 4718592
s_lshl_b32 s50, s87, 1
s_add_u32 s50, s50, 13
s_and_b32 s50, s50, 15
s_lshl_b32 s51, s47, 4
s_add_u32 s50, s50, s51
s_mul_i32 s50, s50, 82048
s_add_u32 s83, s50, 4718592
s_lshl_b32 s50, s87, 1
s_add_u32 s50, s50, 14
s_and_b32 s50, s50, 15
s_lshl_b32 s51, s47, 4
s_add_u32 s50, s50, s51
s_mul_i32 s50, s50, 82048
s_add_u32 s84, s50, 4718592
s_lshl_b32 s50, s87, 1
s_add_u32 s50, s50, 15
s_and_b32 s50, s50, 15
s_lshl_b32 s51, s47, 4
s_add_u32 s50, s50, s51
s_mul_i32 s50, s50, 82048
s_add_u32 s85, s50, 4718592
s_mul_i32 s50, s89, 20
s_mul_i32 s51, s47, 5
s_add_u32 s50, s50, s51
v_min_u32 v16, 4, v0
v_add_u32 v4, s50, v16
s_mul_i32 s51, s89, 54
s_add_u32 s51, s51, s93
s_add_u32 s51, s51, 160
s_cmp_ge_u32 s47, 2
s_cselect_b32 s52, 12, 13
v_and_b32 v16, 15, v0
v_min_u32 v16, s52, v16
v_add_u32 v16, s51, v16
v_cmp_le_u32 vcc, 16, v0
s_lshl_b32 s51, s92, 3
s_add_u32 s51, s51, 592
v_cndmask_b32 v4, v4, v16, vcc
v_and_b32 v16, 7, v0
v_add_u32 v16, s51, v16
s_cmp_ge_u32 s92, 54
s_cselect_b32 s51, 64, 32
v_cmp_le_u32 vcc, s51, v0
s_nop 1
v_cndmask_b32 v4, v4, v16, vcc
v_lshlrev_b32 v4, 7, v4
v_mov_b32 v15, v4
s_barrier
s_waitcnt vmcnt(10)
v_mov_b32 v4, 0
v_mov_b32 v5, 0
v_mfma_f32_32x32x4_2b_bf16 v[16:47], v[168:169], v[160:161], 0
s_nop 0
v_mfma_f32_32x32x8_bf16 v[96:111], v[4:5], v[4:5], 0
v_mfma_f32_32x32x8_bf16 v[112:127], v[4:5], v[4:5], 0
v_mfma_f32_32x32x8_bf16 v[128:143], v[4:5], v[4:5], 0
s_nop 13
v_mfma_f32_32x32x16_bf16 v[16:31], v[164:167], v[144:147], v[16:31]
v_mfma_f32_32x32x16_bf16 v[32:47], v[164:167], v[148:151], v[32:47]
v_mfma_f32_32x32x8_bf16 v[80:95], v[4:5], v[4:5], 0
s_nop 9
s_waitcnt vmcnt(8)
v_exp_f32 v16, v16
v_exp_f32 v17, v17
v_exp_f32 v18, v18
v_exp_f32 v19, v19
v_exp_f32 v20, v20
v_mfma_f32_32x32x4_2b_bf16 v[48:79], v[168:169], v[162:163], 0
buffer_load_dwordx4 v[206:209], v10, s[12:15], s73 offen sc1
v_exp_f32 v21, v21
v_exp_f32 v22, v22
v_exp_f32 v23, v23
v_exp_f32 v24, v24
v_exp_f32 v25, v25
v_exp_f32 v26, v26
v_exp_f32 v27, v27
v_exp_f32 v28, v28
v_smfmac_f32_32x32x32_bf16 v[96:111], v[182:185], v[16:23], v14
buffer_load_dwordx4 v[210:213], v11, s[12:15], s73 offen sc1
v_exp_f32 v29, v29
v_exp_f32 v30, v30
v_exp_f32 v31, v31
v_exp_f32 v32, v32
v_exp_f32 v33, v33
v_smfmac_f32_32x32x32_bf16 v[96:111], v[186:189], v[24:31], v14
v_exp_f32 v34, v34
v_exp_f32 v35, v35
v_exp_f32 v36, v36
v_mfma_f32_32x32x16_bf16 v[48:63], v[164:167], v[152:155], v[48:63]
v_exp_f32 v37, v37
v_exp_f32 v38, v38
v_exp_f32 v39, v39
v_mfma_f32_32x32x16_bf16 v[64:79], v[164:167], v[156:159], v[64:79]
v_exp_f32 v40, v40
v_exp_f32 v41, v41
v_exp_f32 v42, v42
v_smfmac_f32_32x32x32_bf16 v[112:127], v[182:185], v[32:39], v14
v_exp_f32 v43, v43
v_exp_f32 v44, v44
v_exp_f32 v45, v45
v_exp_f32 v46, v46
v_exp_f32 v47, v47
s_waitcnt vmcnt(6)
v_exp_f32 v48, v48
v_exp_f32 v49, v49
v_smfmac_f32_32x32x32_bf16 v[112:127], v[186:189], v[40:47], v14
buffer_load_dwordx4 v[164:167], v8, s[12:15], s73 offen sc1
v_exp_f32 v50, v50
v_exp_f32 v51, v51
v_exp_f32 v52, v52
v_mfma_f32_32x32x4_2b_bf16 v[16:47], v[174:175], v[160:161], 0
buffer_load_dwordx2 v[168:169], v9, s[12:15], s73 offen sc1
v_exp_f32 v53, v53
v_exp_f32 v54, v54
v_exp_f32 v55, v55
v_exp_f32 v56, v56
v_exp_f32 v57, v57
v_exp_f32 v58, v58
v_exp_f32 v59, v59
v_exp_f32 v60, v60
v_smfmac_f32_32x32x32_bf16 v[128:143], v[182:185], v[48:55], v14
)ASM"
      R"ASM(
v_exp_f32 v61, v61
v_exp_f32 v62, v62
v_exp_f32 v63, v63
v_exp_f32 v64, v64
v_exp_f32 v65, v65
v_smfmac_f32_32x32x32_bf16 v[128:143], v[186:189], v[56:63], v14
v_exp_f32 v66, v66
v_exp_f32 v67, v67
v_exp_f32 v68, v68
v_mfma_f32_32x32x16_bf16 v[16:31], v[170:173], v[144:147], v[16:31]
v_exp_f32 v69, v69
v_exp_f32 v70, v70
v_exp_f32 v71, v71
v_mfma_f32_32x32x16_bf16 v[32:47], v[170:173], v[148:151], v[32:47]
v_exp_f32 v72, v72
v_exp_f32 v73, v73
v_exp_f32 v74, v74
v_smfmac_f32_32x32x32_bf16 v[80:95], v[182:185], v[64:71], v14
v_exp_f32 v75, v75
v_exp_f32 v76, v76
v_exp_f32 v77, v77
v_exp_f32 v78, v78
v_exp_f32 v79, v79
v_exp_f32 v16, v16
v_exp_f32 v17, v17
v_smfmac_f32_32x32x32_bf16 v[80:95], v[186:189], v[72:79], v14
buffer_load_dwordx4 v[182:185], v10, s[12:15], s74 offen sc1
v_exp_f32 v18, v18
v_exp_f32 v19, v19
v_exp_f32 v20, v20
v_mfma_f32_32x32x4_2b_bf16 v[48:79], v[174:175], v[162:163], 0
buffer_load_dwordx4 v[186:189], v11, s[12:15], s74 offen sc1
v_exp_f32 v21, v21
v_exp_f32 v22, v22
v_exp_f32 v23, v23
v_exp_f32 v24, v24
v_exp_f32 v25, v25
v_exp_f32 v26, v26
v_exp_f32 v27, v27
v_exp_f32 v28, v28
v_smfmac_f32_32x32x32_bf16 v[96:111], v[190:193], v[16:23], v14
v_exp_f32 v29, v29
v_exp_f32 v30, v30
v_exp_f32 v31, v31
v_exp_f32 v32, v32
v_exp_f32 v33, v33
v_smfmac_f32_32x32x32_bf16 v[96:111], v[194:197], v[24:31], v14
s_mov_b32 s50, 131072
buffer_load_dwordx2 v[6:7], v15, s[12:15], s50 offen sc1
v_exp_f32 v34, v34
v_exp_f32 v35, v35
v_exp_f32 v36, v36
v_mfma_f32_32x32x16_bf16 v[48:63], v[170:173], v[152:155], v[48:63]
v_exp_f32 v37, v37
v_exp_f32 v38, v38
v_exp_f32 v39, v39
v_mfma_f32_32x32x16_bf16 v[64:79], v[170:173], v[156:159], v[64:79]
v_exp_f32 v40, v40
v_exp_f32 v41, v41
v_exp_f32 v42, v42
v_smfmac_f32_32x32x32_bf16 v[112:127], v[190:193], v[32:39], v14
v_exp_f32 v43, v43
v_exp_f32 v44, v44
v_exp_f32 v45, v45
v_exp_f32 v46, v46
v_exp_f32 v47, v47
s_waitcnt vmcnt(7)
v_exp_f32 v48, v48
v_exp_f32 v49, v49
v_smfmac_f32_32x32x32_bf16 v[112:127], v[194:197], v[40:47], v14
buffer_load_dwordx4 v[170:173], v8, s[12:15], s74 offen sc1
v_exp_f32 v50, v50
v_exp_f32 v51, v51
v_exp_f32 v52, v52
v_mfma_f32_32x32x4_2b_bf16 v[16:47], v[180:181], v[160:161], 0
buffer_load_dwordx2 v[174:175], v9, s[12:15], s74 offen sc1
v_exp_f32 v53, v53
v_exp_f32 v54, v54
v_exp_f32 v55, v55
v_exp_f32 v56, v56
v_exp_f32 v57, v57
v_exp_f32 v58, v58
v_exp_f32 v59, v59
v_exp_f32 v60, v60
v_smfmac_f32_32x32x32_bf16 v[128:143], v[190:193], v[48:55], v14
v_exp_f32 v61, v61
v_exp_f32 v62, v62
v_exp_f32 v63, v63
v_exp_f32 v64, v64
v_exp_f32 v65, v65
v_smfmac_f32_32x32x32_bf16 v[128:143], v[194:197], v[56:63], v14
v_exp_f32 v66, v66
v_exp_f32 v67, v67
v_exp_f32 v68, v68
v_mfma_f32_32x32x16_bf16 v[16:31], v[176:179], v[144:147], v[16:31]
v_exp_f32 v69, v69
v_exp_f32 v70, v70
v_exp_f32 v71, v71
v_mfma_f32_32x32x16_bf16 v[32:47], v[176:179], v[148:151], v[32:47]
v_exp_f32 v72, v72
v_exp_f32 v73, v73
v_exp_f32 v74, v74
v_smfmac_f32_32x32x32_bf16 v[80:95], v[190:193], v[64:71], v14
v_exp_f32 v75, v75
v_exp_f32 v76, v76
v_exp_f32 v77, v77
v_exp_f32 v78, v78
v_exp_f32 v79, v79
v_exp_f32 v16, v16
v_exp_f32 v17, v17
v_smfmac_f32_32x32x32_bf16 v[80:95], v[194:197], v[72:79], v14
buffer_load_dwordx4 v[190:193], v10, s[12:15], s75 offen sc1
v_exp_f32 v18, v18
v_exp_f32 v19, v19
v_exp_f32 v20, v20
v_mfma_f32_32x32x4_2b_bf16 v[48:79], v[180:181], v[162:163], 0
buffer_load_dwordx4 v[194:197], v11, s[12:15], s75 offen sc1
v_exp_f32 v21, v21
v_exp_f32 v22, v22
v_exp_f32 v23, v23
v_exp_f32 v24, v24
v_exp_f32 v25, v25
v_exp_f32 v26, v26
v_exp_f32 v27, v27
v_exp_f32 v28, v28
v_smfmac_f32_32x32x32_bf16 v[96:111], v[198:201], v[16:23], v14
v_exp_f32 v29, v29
v_exp_f32 v30, v30
v_exp_f32 v31, v31
v_exp_f32 v32, v32
v_exp_f32 v33, v33
v_smfmac_f32_32x32x32_bf16 v[96:111], v[202:205], v[24:31], v14
v_exp_f32 v34, v34
v_exp_f32 v35, v35
v_exp_f32 v36, v36
v_mfma_f32_32x32x16_bf16 v[48:63], v[176:179], v[152:155], v[48:63]
v_exp_f32 v37, v37
v_exp_f32 v38, v38
v_exp_f32 v39, v39
v_mfma_f32_32x32x16_bf16 v[64:79], v[176:179], v[156:159], v[64:79]
v_exp_f32 v40, v40
v_exp_f32 v41, v41
v_exp_f32 v42, v42
v_smfmac_f32_32x32x32_bf16 v[112:127], v[198:201], v[32:39], v14
v_exp_f32 v43, v43
v_exp_f32 v44, v44
v_exp_f32 v45, v45
v_exp_f32 v46, v46
v_exp_f32 v47, v47
s_waitcnt vmcnt(7)
v_exp_f32 v48, v48
v_exp_f32 v49, v49
v_smfmac_f32_32x32x32_bf16 v[112:127], v[202:205], v[40:47], v14
buffer_load_dwordx4 v[176:179], v8, s[12:15], s75 offen sc1
v_exp_f32 v50, v50
v_exp_f32 v51, v51
v_exp_f32 v52, v52
v_mfma_f32_32x32x4_2b_bf16 v[16:47], v[168:169], v[160:161], 0
buffer_load_dwordx2 v[180:181], v9, s[12:15], s75 offen sc1
v_exp_f32 v53, v53
v_exp_f32 v54, v54
v_exp_f32 v55, v55
v_exp_f32 v56, v56
v_exp_f32 v57, v57
v_exp_f32 v58, v58
v_exp_f32 v59, v59
v_exp_f32 v60, v60
v_smfmac_f32_32x32x32_bf16 v[128:143], v[198:201], v[48:55], v14
v_exp_f32 v61, v61
v_exp_f32 v62, v62
v_exp_f32 v63, v63
v_exp_f32 v64, v64
v_exp_f32 v65, v65
v_smfmac_f32_32x32x32_bf16 v[128:143], v[202:205], v[56:63], v14
v_exp_f32 v66, v66
v_exp_f32 v67, v67
v_exp_f32 v68, v68
v_mfma_f32_32x32x16_bf16 v[16:31], v[164:167], v[144:147], v[16:31]
v_exp_f32 v69, v69
v_exp_f32 v70, v70
v_exp_f32 v71, v71
v_mfma_f32_32x32x16_bf16 v[32:47], v[164:167], v[148:151], v[32:47]
v_exp_f32 v72, v72
v_exp_f32 v73, v73
v_exp_f32 v74, v74
v_smfmac_f32_32x32x32_bf16 v[80:95], v[198:201], v[64:71], v14
v_exp_f32 v75, v75
v_exp_f32 v76, v76
v_exp_f32 v77, v77
v_exp_f32 v78, v78
v_exp_f32 v79, v79
s_waitcnt vmcnt(6)
v_cmp_ne_u64 vcc, s[44:45], v[6:7]
s_mov_b32 m0, s67
s_cbranch_vccz .Lmk_fwok_25
s_mov_b32 s50, 131072
.Lmk_poll_26:
buffer_load_dwordx2 v[6:7], v15, s[12:15], s50 offen sc1
s_waitcnt vmcnt(0)
v_cmp_ne_u64 vcc, s[44:45], v[6:7]
s_nop 0
s_cbranch_vccz .Lmk_polld_27
s_sub_u32 s46, s46, 1
s_cbranch_scc0 .Lmk_poll_26
s_mov_b32 s46, 0
.Lmk_polld_27:
.Lmk_fwok_25:
v_exp_f32 v16, v16
v_exp_f32 v17, v17
v_smfmac_f32_32x32x32_bf16 v[80:95], v[202:205], v[72:79], v14
buffer_load_dwordx4 v[198:201], v10, s[12:15], s76 offen sc1
v_exp_f32 v18, v18
v_exp_f32 v19, v19
v_exp_f32 v20, v20
v_mfma_f32_32x32x4_2b_bf16 v[48:79], v[168:169], v[162:163], 0
buffer_load_dwordx4 v[202:205], v11, s[12:15], s76 offen sc1
v_exp_f32 v21, v21
v_exp_f32 v22, v22
v_exp_f32 v23, v23
v_exp_f32 v24, v24
v_exp_f32 v25, v25
v_exp_f32 v26, v26
v_exp_f32 v27, v27
v_exp_f32 v28, v28
v_smfmac_f32_32x32x32_bf16 v[96:111], v[206:209], v[16:23], v14
v_exp_f32 v29, v29
v_exp_f32 v30, v30
v_exp_f32 v31, v31
v_exp_f32 v32, v32
v_exp_f32 v33, v33
v_smfmac_f32_32x32x32_bf16 v[96:111], v[210:213], v[24:31], v14
buffer_load_dwordx4 v2, s[12:15], s66 offen offset:0 sc1 lds
v_exp_f32 v34, v34
v_exp_f32 v35, v35
v_exp_f32 v36, v36
v_mfma_f32_32x32x16_bf16 v[48:63], v[164:167], v[152:155], v[48:63]
v_exp_f32 v37, v37
v_exp_f32 v38, v38
v_exp_f32 v39, v39
v_mfma_f32_32x32x16_bf16 v[64:79], v[164:167], v[156:159], v[64:79]
buffer_load_dwordx4 v2, s[12:15], s66 offen offset:1024 sc1 lds
v_exp_f32 v40, v40
v_exp_f32 v41, v41
v_exp_f32 v42, v42
v_smfmac_f32_32x32x32_bf16 v[112:127], v[206:209], v[32:39], v14
v_exp_f32 v43, v43
v_exp_f32 v44, v44
v_exp_f32 v45, v45
v_exp_f32 v46, v46
v_exp_f32 v47, v47
s_waitcnt vmcnt(8)
v_exp_f32 v48, v48
v_exp_f32 v49, v49
v_smfmac_f32_32x32x32_bf16 v[112:127], v[210:213], v[40:47], v14
buffer_load_dwordx4 v[164:167], v8, s[12:15], s76 offen sc1
v_exp_f32 v50, v50
v_exp_f32 v51, v51
v_exp_f32 v52, v52
v_mfma_f32_32x32x4_2b_bf16 v[16:47], v[174:175], v[160:161], 0
buffer_load_dwordx2 v[168:169], v9, s[12:15], s76 offen sc1
v_exp_f32 v53, v53
v_exp_f32 v54, v54
v_exp_f32 v55, v55
v_exp_f32 v56, v56
v_exp_f32 v57, v57
v_exp_f32 v58, v58
v_exp_f32 v59, v59
v_exp_f32 v60, v60
v_smfmac_f32_32x32x32_bf16 v[128:143], v[206:209], v[48:55], v14
v_exp_f32 v61, v61
v_exp_f32 v62, v62
v_exp_f32 v63, v63
v_exp_f32 v64, v64
v_exp_f32 v65, v65
v_smfmac_f32_32x32x32_bf16 v[128:143], v[210:213], v[56:63], v14
buffer_load_dwordx4 v2, s[12:15], s66 offen offset:2048 sc1 lds
v_exp_f32 v66, v66
v_exp_f32 v67, v67
v_exp_f32 v68, v68
v_mfma_f32_32x32x16_bf16 v[16:31], v[170:173], v[144:147], v[16:31]
v_exp_f32 v69, v69
v_exp_f32 v70, v70
v_exp_f32 v71, v71
v_mfma_f32_32x32x16_bf16 v[32:47], v[170:173], v[148:151], v[32:47]
buffer_load_dwordx4 v2, s[12:15], s66 offen offset:3072 sc1 lds
s_add_u32 s66, s66, 4096
s_add_u32 m0, m0, 4096
v_exp_f32 v72, v72
v_exp_f32 v73, v73
v_exp_f32 v74, v74
v_smfmac_f32_32x32x32_bf16 v[80:95], v[206:209], v[64:71], v14
v_exp_f32 v75, v75
v_exp_f32 v76, v76
v_exp_f32 v77, v77
v_exp_f32 v78, v78
v_exp_f32 v79, v79
v_exp_f32 v16, v16
v_exp_f32 v17, v17
v_smfmac_f32_32x32x32_bf16 v[80:95], v[210:213], v[72:79], v14
buffer_load_dwordx4 v[206:209], v10, s[12:15], s77 offen sc1
v_exp_f32 v18, v18
v_exp_f32 v19, v19
v_exp_f32 v20, v20
v_mfma_f32_32x32x4_2b_bf16 v[48:79], v[174:175], v[162:163], 0
buffer_load_dwordx4 v[210:213], v11, s[12:15], s77 offen sc1
v_exp_f32 v21, v21
v_exp_f32 v22, v22
v_exp_f32 v23, v23
v_exp_f32 v24, v24
v_exp_f32 v25, v25
v_exp_f32 v26, v26
v_exp_f32 v27, v27
v_exp_f32 v28, v28
v_smfmac_f32_32x32x32_bf16 v[96:111], v[182:185], v[16:23], v14
v_exp_f32 v29, v29
v_exp_f32 v30, v30
v_exp_f32 v31, v31
v_exp_f32 v32, v32
v_exp_f32 v33, v33
v_smfmac_f32_32x32x32_bf16 v[96:111], v[186:189], v[24:31], v14
buffer_load_dwordx4 v2, s[12:15], s66 offen offset:0 sc1 lds
v_exp_f32 v34, v34
v_exp_f32 v35, v35
v_exp_f32 v36, v36
v_mfma_f32_32x32x16_bf16 v[48:63], v[170:173], v[152:155], v[48:63]
v_exp_f32 v37, v37
v_exp_f32 v38, v38
v_exp_f32 v39, v39
v_mfma_f32_32x32x16_bf16 v[64:79], v[170:173], v[156:159], v[64:79]
buffer_load_dwordx4 v2, s[12:15], s66 offen offset:1024 sc1 lds
v_exp_f32 v40, v40
v_exp_f32 v41, v41
v_exp_f32 v42, v42
v_smfmac_f32_32x32x32_bf16 v[112:127], v[182:185], v[32:39], v14
v_exp_f32 v43, v43
v_exp_f32 v44, v44
v_exp_f32 v45, v45
v_exp_f32 v46, v46
v_exp_f32 v47, v47
s_waitcnt vmcnt(12)
v_exp_f32 v48, v48
v_exp_f32 v49, v49
v_smfmac_f32_32x32x32_bf16 v[112:127], v[186:189], v[40:47], v14
buffer_load_dwordx4 v[170:173], v8, s[12:15], s77 offen sc1
v_exp_f32 v50, v50
v_exp_f32 v51, v51
v_exp_f32 v52, v52
v_mfma_f32_32x32x4_2b_bf16 v[16:47], v[180:181], v[160:161], 0
buffer_load_dwordx2 v[174:175], v9, s[12:15], s77 offen sc1
v_exp_f32 v53, v53
v_exp_f32 v54, v54
v_exp_f32 v55, v55
v_exp_f32 v56, v56
v_exp_f32 v57, v57
v_exp_f32 v58, v58
v_exp_f32 v59, v59
v_exp_f32 v60, v60
v_smfmac_f32_32x32x32_bf16 v[128:143], v[182:185], v[48:55], v14
v_exp_f32 v61, v61
v_exp_f32 v62, v62
v_exp_f32 v63, v63
v_exp_f32 v64, v64
v_exp_f32 v65, v65
v_smfmac_f32_32x32x32_bf16 v[128:143], v[186:189], v[56:63], v14
buffer_load_dwordx4 v2, s[12:15], s66 offen offset:2048 sc1 lds
v_exp_f32 v66, v66
v_exp_f32 v67, v67
v_exp_f32 v68, v68
v_mfma_f32_32x32x16_bf16 v[16:31], v[176:179], v[144:147], v[16:31]
v_exp_f32 v69, v69
v_exp_f32 v70, v70
v_exp_f32 v71, v71
v_mfma_f32_32x32x16_bf16 v[32:47], v[176:179], v[148:151], v[32:47]
buffer_load_dwordx4 v2, s[12:15], s66 offen offset:3072 sc1 lds
s_add_u32 s66, s66, 4096
s_add_u32 m0, m0, 4096
v_exp_f32 v72, v72
v_exp_f32 v73, v73
v_exp_f32 v74, v74
v_smfmac_f32_32x32x32_bf16 v[80:95], v[182:185], v[64:71], v14
v_exp_f32 v75, v75
v_exp_f32 v76, v76
v_exp_f32 v77, v77
v_exp_f32 v78, v78
v_exp_f32 v79, v79
v_exp_f32 v16, v16
v_exp_f32 v17, v17
v_smfmac_f32_32x32x32_bf16 v[80:95], v[186:189], v[72:79], v14
buffer_load_dwordx4 v[182:185], v10, s[12:15], s78 offen sc1
v_exp_f32 v18, v18
v_exp_f32 v19, v19
v_exp_f32 v20, v20
v_mfma_f32_32x32x4_2b_bf16 v[48:79], v[180:181], v[162:163], 0
buffer_load_dwordx4 v[186:189], v11, s[12:15], s78 offen sc1
v_exp_f32 v21, v21
v_exp_f32 v22, v22
v_exp_f32 v23, v23
v_exp_f32 v24, v24
v_exp_f32 v25, v25
v_exp_f32 v26, v26
)ASM"
      R"ASM(
v_exp_f32 v27, v27
v_exp_f32 v28, v28
v_smfmac_f32_32x32x32_bf16 v[96:111], v[190:193], v[16:23], v14
v_exp_f32 v29, v29
v_exp_f32 v30, v30
v_exp_f32 v31, v31
v_exp_f32 v32, v32
v_exp_f32 v33, v33
v_smfmac_f32_32x32x32_bf16 v[96:111], v[194:197], v[24:31], v14
buffer_load_dwordx4 v2, s[12:15], s66 offen offset:0 sc1 lds
v_exp_f32 v34, v34
v_exp_f32 v35, v35
v_exp_f32 v36, v36
v_mfma_f32_32x32x16_bf16 v[48:63], v[176:179], v[152:155], v[48:63]
v_exp_f32 v37, v37
v_exp_f32 v38, v38
v_exp_f32 v39, v39
v_mfma_f32_32x32x16_bf16 v[64:79], v[176:179], v[156:159], v[64:79]
buffer_load_dwordx4 v2, s[12:15], s66 offen offset:1024 sc1 lds
v_exp_f32 v40, v40
v_exp_f32 v41, v41
v_exp_f32 v42, v42
v_smfmac_f32_32x32x32_bf16 v[112:127], v[190:193], v[32:39], v14
v_exp_f32 v43, v43
v_exp_f32 v44, v44
v_exp_f32 v45, v45
v_exp_f32 v46, v46
v_exp_f32 v47, v47
s_waitcnt vmcnt(14)
v_exp_f32 v48, v48
v_exp_f32 v49, v49
v_smfmac_f32_32x32x32_bf16 v[112:127], v[194:197], v[40:47], v14
buffer_load_dwordx4 v[176:179], v8, s[12:15], s78 offen sc1
v_exp_f32 v50, v50
v_exp_f32 v51, v51
v_exp_f32 v52, v52
v_mfma_f32_32x32x4_2b_bf16 v[16:47], v[168:169], v[160:161], 0
buffer_load_dwordx2 v[180:181], v9, s[12:15], s78 offen sc1
v_exp_f32 v53, v53
v_exp_f32 v54, v54
v_exp_f32 v55, v55
v_exp_f32 v56, v56
v_exp_f32 v57, v57
v_exp_f32 v58, v58
v_exp_f32 v59, v59
v_exp_f32 v60, v60
v_smfmac_f32_32x32x32_bf16 v[128:143], v[190:193], v[48:55], v14
v_exp_f32 v61, v61
v_exp_f32 v62, v62
v_exp_f32 v63, v63
v_exp_f32 v64, v64
v_exp_f32 v65, v65
v_smfmac_f32_32x32x32_bf16 v[128:143], v[194:197], v[56:63], v14
buffer_load_dwordx4 v2, s[12:15], s66 offen offset:2048 sc1 lds
v_exp_f32 v66, v66
v_exp_f32 v67, v67
v_exp_f32 v68, v68
v_mfma_f32_32x32x16_bf16 v[16:31], v[164:167], v[144:147], v[16:31]
v_exp_f32 v69, v69
v_exp_f32 v70, v70
v_exp_f32 v71, v71
v_mfma_f32_32x32x16_bf16 v[32:47], v[164:167], v[148:151], v[32:47]
buffer_load_dwordx4 v2, s[12:15], s66 offen offset:3072 sc1 lds
s_add_u32 s66, s66, 4096
s_add_u32 m0, m0, 4096
v_exp_f32 v72, v72
v_exp_f32 v73, v73
v_exp_f32 v74, v74
v_smfmac_f32_32x32x32_bf16 v[80:95], v[190:193], v[64:71], v14
v_exp_f32 v75, v75
v_exp_f32 v76, v76
v_exp_f32 v77, v77
v_exp_f32 v78, v78
v_exp_f32 v79, v79
v_exp_f32 v16, v16
v_exp_f32 v17, v17
v_smfmac_f32_32x32x32_bf16 v[80:95], v[194:197], v[72:79], v14
buffer_load_dwordx4 v[190:193], v10, s[12:15], s79 offen sc1
v_exp_f32 v18, v18
v_exp_f32 v19, v19
v_exp_f32 v20, v20
v_mfma_f32_32x32x4_2b_bf16 v[48:79], v[168:169], v[162:163], 0
buffer_load_dwordx4 v[194:197], v11, s[12:15], s79 offen sc1
v_exp_f32 v21, v21
v_exp_f32 v22, v22
v_exp_f32 v23, v23
v_exp_f32 v24, v24
v_exp_f32 v25, v25
v_exp_f32 v26, v26
v_exp_f32 v27, v27
v_exp_f32 v28, v28
v_smfmac_f32_32x32x32_bf16 v[96:111], v[198:201], v[16:23], v14
v_exp_f32 v29, v29
v_exp_f32 v30, v30
v_exp_f32 v31, v31
v_exp_f32 v32, v32
v_exp_f32 v33, v33
v_smfmac_f32_32x32x32_bf16 v[96:111], v[202:205], v[24:31], v14
buffer_load_dwordx4 v2, s[12:15], s66 offen offset:0 sc1 lds
v_exp_f32 v34, v34
v_exp_f32 v35, v35
v_exp_f32 v36, v36
v_mfma_f32_32x32x16_bf16 v[48:63], v[164:167], v[152:155], v[48:63]
v_exp_f32 v37, v37
v_exp_f32 v38, v38
v_exp_f32 v39, v39
v_mfma_f32_32x32x16_bf16 v[64:79], v[164:167], v[156:159], v[64:79]
buffer_load_dwordx4 v2, s[12:15], s66 offen offset:1024 sc1 lds
v_exp_f32 v40, v40
v_exp_f32 v41, v41
v_exp_f32 v42, v42
v_smfmac_f32_32x32x32_bf16 v[112:127], v[198:201], v[32:39], v14
v_exp_f32 v43, v43
v_exp_f32 v44, v44
v_exp_f32 v45, v45
v_exp_f32 v46, v46
v_exp_f32 v47, v47
s_waitcnt vmcnt(14)
v_exp_f32 v48, v48
v_exp_f32 v49, v49
v_smfmac_f32_32x32x32_bf16 v[112:127], v[202:205], v[40:47], v14
buffer_load_dwordx4 v[164:167], v8, s[12:15], s79 offen sc1
v_exp_f32 v50, v50
v_exp_f32 v51, v51
v_exp_f32 v52, v52
v_mfma_f32_32x32x4_2b_bf16 v[16:47], v[174:175], v[160:161], 0
buffer_load_dwordx2 v[168:169], v9, s[12:15], s79 offen sc1
v_exp_f32 v53, v53
v_exp_f32 v54, v54
v_exp_f32 v55, v55
v_exp_f32 v56, v56
v_exp_f32 v57, v57
v_exp_f32 v58, v58
v_exp_f32 v59, v59
v_exp_f32 v60, v60
v_smfmac_f32_32x32x32_bf16 v[128:143], v[198:201], v[48:55], v14
v_exp_f32 v61, v61
v_exp_f32 v62, v62
v_exp_f32 v63, v63
v_exp_f32 v64, v64
v_exp_f32 v65, v65
v_smfmac_f32_32x32x32_bf16 v[128:143], v[202:205], v[56:63], v14
buffer_load_dwordx4 v2, s[12:15], s66 offen offset:2048 sc1 lds
v_exp_f32 v66, v66
v_exp_f32 v67, v67
v_exp_f32 v68, v68
v_mfma_f32_32x32x16_bf16 v[16:31], v[170:173], v[144:147], v[16:31]
v_exp_f32 v69, v69
v_exp_f32 v70, v70
v_exp_f32 v71, v71
v_mfma_f32_32x32x16_bf16 v[32:47], v[170:173], v[148:151], v[32:47]
buffer_load_dwordx4 v2, s[12:15], s66 offen offset:3072 sc1 lds
s_add_u32 s66, s66, 4096
s_add_u32 m0, m0, 4096
v_exp_f32 v72, v72
v_exp_f32 v73, v73
v_exp_f32 v74, v74
v_smfmac_f32_32x32x32_bf16 v[80:95], v[198:201], v[64:71], v14
v_exp_f32 v75, v75
v_exp_f32 v76, v76
v_exp_f32 v77, v77
v_exp_f32 v78, v78
v_exp_f32 v79, v79
v_exp_f32 v16, v16
v_exp_f32 v17, v17
v_smfmac_f32_32x32x32_bf16 v[80:95], v[202:205], v[72:79], v14
buffer_load_dwordx4 v[198:201], v10, s[12:15], s80 offen sc1
v_exp_f32 v18, v18
v_exp_f32 v19, v19
v_exp_f32 v20, v20
v_mfma_f32_32x32x4_2b_bf16 v[48:79], v[174:175], v[162:163], 0
buffer_load_dwordx4 v[202:205], v11, s[12:15], s80 offen sc1
v_exp_f32 v21, v21
v_exp_f32 v22, v22
v_exp_f32 v23, v23
v_exp_f32 v24, v24
v_exp_f32 v25, v25
v_exp_f32 v26, v26
v_exp_f32 v27, v27
v_exp_f32 v28, v28
v_smfmac_f32_32x32x32_bf16 v[96:111], v[206:209], v[16:23], v14
v_exp_f32 v29, v29
v_exp_f32 v30, v30
v_exp_f32 v31, v31
v_exp_f32 v32, v32
v_exp_f32 v33, v33
v_smfmac_f32_32x32x32_bf16 v[96:111], v[210:213], v[24:31], v14
buffer_load_dwordx4 v2, s[12:15], s66 offen offset:0 sc1 lds
v_exp_f32 v34, v34
v_exp_f32 v35, v35
v_exp_f32 v36, v36
v_mfma_f32_32x32x16_bf16 v[48:63], v[170:173], v[152:155], v[48:63]
v_exp_f32 v37, v37
v_exp_f32 v38, v38
v_exp_f32 v39, v39
v_mfma_f32_32x32x16_bf16 v[64:79], v[170:173], v[156:159], v[64:79]
buffer_load_dwordx4 v2, s[12:15], s66 offen offset:1024 sc1 lds
v_exp_f32 v40, v40
v_exp_f32 v41, v41
v_exp_f32 v42, v42
v_smfmac_f32_32x32x32_bf16 v[112:127], v[206:209], v[32:39], v14
v_exp_f32 v43, v43
v_exp_f32 v44, v44
v_exp_f32 v45, v45
v_exp_f32 v46, v46
v_exp_f32 v47, v47
s_waitcnt vmcnt(14)
v_exp_f32 v48, v48
v_exp_f32 v49, v49
v_smfmac_f32_32x32x32_bf16 v[112:127], v[210:213], v[40:47], v14
buffer_load_dwordx4 v[170:173], v8, s[12:15], s80 offen sc1
v_exp_f32 v50, v50
v_exp_f32 v51, v51
v_exp_f32 v52, v52
v_mfma_f32_32x32x4_2b_bf16 v[16:47], v[180:181], v[160:161], 0
buffer_load_dwordx2 v[174:175], v9, s[12:15], s80 offen sc1
v_exp_f32 v53, v53
v_exp_f32 v54, v54
v_exp_f32 v55, v55
v_exp_f32 v56, v56
v_exp_f32 v57, v57
v_exp_f32 v58, v58
v_exp_f32 v59, v59
v_exp_f32 v60, v60
v_smfmac_f32_32x32x32_bf16 v[128:143], v[206:209], v[48:55], v14
v_exp_f32 v61, v61
v_exp_f32 v62, v62
v_exp_f32 v63, v63
v_exp_f32 v64, v64
v_exp_f32 v65, v65
v_smfmac_f32_32x32x32_bf16 v[128:143], v[210:213], v[56:63], v14
buffer_load_dwordx4 v2, s[12:15], s66 offen offset:2048 sc1 lds
v_exp_f32 v66, v66
v_exp_f32 v67, v67
v_exp_f32 v68, v68
v_mfma_f32_32x32x16_bf16 v[16:31], v[176:179], v[144:147], v[16:31]
v_exp_f32 v69, v69
v_exp_f32 v70, v70
v_exp_f32 v71, v71
v_mfma_f32_32x32x16_bf16 v[32:47], v[176:179], v[148:151], v[32:47]
buffer_load_dwordx4 v2, s[12:15], s66 offen offset:3072 sc1 lds
s_add_u32 s66, s66, 4096
s_add_u32 m0, m0, 4096
v_exp_f32 v72, v72
v_exp_f32 v73, v73
v_exp_f32 v74, v74
v_smfmac_f32_32x32x32_bf16 v[80:95], v[206:209], v[64:71], v14
v_exp_f32 v75, v75
v_exp_f32 v76, v76
v_exp_f32 v77, v77
v_exp_f32 v78, v78
v_exp_f32 v79, v79
v_exp_f32 v16, v16
v_exp_f32 v17, v17
v_smfmac_f32_32x32x32_bf16 v[80:95], v[210:213], v[72:79], v14
buffer_load_dwordx4 v[206:209], v10, s[12:15], s81 offen sc1
v_exp_f32 v18, v18
v_exp_f32 v19, v19
v_exp_f32 v20, v20
v_mfma_f32_32x32x4_2b_bf16 v[48:79], v[180:181], v[162:163], 0
buffer_load_dwordx4 v[210:213], v11, s[12:15], s81 offen sc1
v_exp_f32 v21, v21
v_exp_f32 v22, v22
v_exp_f32 v23, v23
v_exp_f32 v24, v24
v_exp_f32 v25, v25
v_exp_f32 v26, v26
v_exp_f32 v27, v27
v_exp_f32 v28, v28
v_smfmac_f32_32x32x32_bf16 v[96:111], v[182:185], v[16:23], v14
v_exp_f32 v29, v29
v_exp_f32 v30, v30
v_exp_f32 v31, v31
v_exp_f32 v32, v32
v_exp_f32 v33, v33
v_smfmac_f32_32x32x32_bf16 v[96:111], v[186:189], v[24:31], v14
buffer_load_dwordx4 v2, s[12:15], s66 offen offset:0 sc1 lds
v_exp_f32 v34, v34
v_exp_f32 v35, v35
v_exp_f32 v36, v36
v_mfma_f32_32x32x16_bf16 v[48:63], v[176:179], v[152:155], v[48:63]
v_exp_f32 v37, v37
v_exp_f32 v38, v38
v_exp_f32 v39, v39
v_mfma_f32_32x32x16_bf16 v[64:79], v[176:179], v[156:159], v[64:79]
buffer_load_dwordx4 v2, s[12:15], s66 offen offset:1024 sc1 lds
v_exp_f32 v40, v40
v_exp_f32 v41, v41
v_exp_f32 v42, v42
v_smfmac_f32_32x32x32_bf16 v[112:127], v[182:185], v[32:39], v14
v_exp_f32 v43, v43
v_exp_f32 v44, v44
v_exp_f32 v45, v45
v_exp_f32 v46, v46
v_exp_f32 v47, v47
s_waitcnt vmcnt(14)
v_exp_f32 v48, v48
v_exp_f32 v49, v49
v_smfmac_f32_32x32x32_bf16 v[112:127], v[186:189], v[40:47], v14
buffer_load_dwordx4 v[176:179], v8, s[12:15], s81 offen sc1
v_exp_f32 v50, v50
v_exp_f32 v51, v51
v_exp_f32 v52, v52
v_mfma_f32_32x32x4_2b_bf16 v[16:47], v[168:169], v[160:161], 0
buffer_load_dwordx2 v[180:181], v9, s[12:15], s81 offen sc1
v_exp_f32 v53, v53
v_exp_f32 v54, v54
v_exp_f32 v55, v55
v_exp_f32 v56, v56
v_exp_f32 v57, v57
v_exp_f32 v58, v58
v_exp_f32 v59, v59
v_exp_f32 v60, v60
v_smfmac_f32_32x32x32_bf16 v[128:143], v[182:185], v[48:55], v14
v_exp_f32 v61, v61
v_exp_f32 v62, v62
v_exp_f32 v63, v63
v_exp_f32 v64, v64
v_exp_f32 v65, v65
v_smfmac_f32_32x32x32_bf16 v[128:143], v[186:189], v[56:63], v14
buffer_load_dwordx4 v2, s[12:15], s66 offen offset:2048 sc1 lds
v_exp_f32 v66, v66
v_exp_f32 v67, v67
v_exp_f32 v68, v68
v_mfma_f32_32x32x16_bf16 v[16:31], v[164:167], v[144:147], v[16:31]
v_exp_f32 v69, v69
v_exp_f32 v70, v70
v_exp_f32 v71, v71
v_mfma_f32_32x32x16_bf16 v[32:47], v[164:167], v[148:151], v[32:47]
v_exp_f32 v72, v72
v_exp_f32 v73, v73
v_exp_f32 v74, v74
v_smfmac_f32_32x32x32_bf16 v[80:95], v[182:185], v[64:71], v14
v_exp_f32 v75, v75
v_exp_f32 v76, v76
v_exp_f32 v77, v77
v_exp_f32 v78, v78
v_exp_f32 v79, v79
v_exp_f32 v16, v16
v_exp_f32 v17, v17
v_smfmac_f32_32x32x32_bf16 v[80:95], v[186:189], v[72:79], v14
buffer_load_dwordx4 v[182:185], v10, s[12:15], s82 offen sc1
v_exp_f32 v18, v18
v_exp_f32 v19, v19
v_exp_f32 v20, v20
v_mfma_f32_32x32x4_2b_bf16 v[48:79], v[168:169], v[162:163], 0
buffer_load_dwordx4 v[186:189], v11, s[12:15], s82 offen sc1
v_exp_f32 v21, v21
v_exp_f32 v22, v22
v_exp_f32 v23, v23
v_exp_f32 v24, v24
v_exp_f32 v25, v25
v_exp_f32 v26, v26
v_exp_f32 v27, v27
v_exp_f32 v28, v28
v_smfmac_f32_32x32x32_bf16 v[96:111], v[190:193], v[16:23], v14
v_exp_f32 v29, v29
v_exp_f32 v30, v30
v_exp_f32 v31, v31
v_exp_f32 v32, v32
v_exp_f32 v33, v33
v_smfmac_f32_32x32x32_bf16 v[96:111], v[194:197], v[24:31], v14
buffer_load_dwordx4 v2, s[12:15], s66 offen offset:3072 sc1 lds
s_add_u32 s66, s66, 4096
s_add_u32 m0, m0, 4096
v_exp_f32 v34, v34
v_exp_f32 v35, v35
v_exp_f32 v36, v36
v_mfma_f32_32x32x16_bf16 v[48:63], v[164:167], v[152:155], v[48:63]
v_exp_f32 v37, v37
v_exp_f32 v38, v38
v_exp_f32 v39, v39
v_mfma_f32_32x32x16_bf16 v[64:79], v[164:167], v[156:159], v[64:79]
buffer_load_dwordx4 v2, s[12:15], s66 offen offset:0 sc1 lds
v_exp_f32 v40, v40
v_exp_f32 v41, v41
v_exp_f32 v42, v42
v_smfmac_f32_32x32x32_bf16 v[112:127], v[190:193], v[32:39], v14
v_exp_f32 v43, v43
v_exp_f32 v44, v44
v_exp_f32 v45, v45
v_exp_f32 v46, v46
v_exp_f32 v47, v47
s_waitcnt vmcnt(13)
v_exp_f32 v48, v48
v_exp_f32 v49, v49
v_smfmac_f32_32x32x32_bf16 v[112:127], v[194:197], v[40:47], v14
buffer_load_dwordx4 v[164:167], v8, s[12:15], s82 offen sc1
v_exp_f32 v50, v50
v_exp_f32 v51, v51
v_exp_f32 v52, v52
v_mfma_f32_32x32x4_2b_bf16 v[16:47], v[174:175], v[160:161], 0
buffer_load_dwordx2 v[168:169], v9, s[12:15], s82 offen sc1
v_exp_f32 v53, v53
v_exp_f32 v54, v54
v_exp_f32 v55, v55
v_exp_f32 v56, v56
v_exp_f32 v57, v57
v_exp_f32 v58, v58
v_exp_f32 v59, v59
)ASM"
      R"ASM(
v_exp_f32 v60, v60
v_smfmac_f32_32x32x32_bf16 v[128:143], v[190:193], v[48:55], v14
v_exp_f32 v61, v61
v_exp_f32 v62, v62
v_exp_f32 v63, v63
v_exp_f32 v64, v64
v_exp_f32 v65, v65
v_smfmac_f32_32x32x32_bf16 v[128:143], v[194:197], v[56:63], v14
buffer_load_dwordx4 a[0:3], v2, s[12:15], s64 offen offset:0 sc1
v_exp_f32 v66, v66
v_exp_f32 v67, v67
v_exp_f32 v68, v68
v_mfma_f32_32x32x16_bf16 v[16:31], v[170:173], v[144:147], v[16:31]
v_exp_f32 v69, v69
v_exp_f32 v70, v70
v_exp_f32 v71, v71
v_mfma_f32_32x32x16_bf16 v[32:47], v[170:173], v[148:151], v[32:47]
buffer_load_dwordx4 a[4:7], v2, s[12:15], s64 offen offset:1024 sc1
v_exp_f32 v72, v72
v_exp_f32 v73, v73
v_exp_f32 v74, v74
v_smfmac_f32_32x32x32_bf16 v[80:95], v[190:193], v[64:71], v14
v_exp_f32 v75, v75
v_exp_f32 v76, v76
v_exp_f32 v77, v77
v_exp_f32 v78, v78
v_exp_f32 v79, v79
v_exp_f32 v16, v16
v_exp_f32 v17, v17
v_smfmac_f32_32x32x32_bf16 v[80:95], v[194:197], v[72:79], v14
buffer_load_dwordx4 v[190:193], v10, s[12:15], s83 offen sc1
v_exp_f32 v18, v18
v_exp_f32 v19, v19
v_exp_f32 v20, v20
v_mfma_f32_32x32x4_2b_bf16 v[48:79], v[174:175], v[162:163], 0
buffer_load_dwordx4 v[194:197], v11, s[12:15], s83 offen sc1
v_exp_f32 v21, v21
v_exp_f32 v22, v22
v_exp_f32 v23, v23
v_exp_f32 v24, v24
v_exp_f32 v25, v25
v_exp_f32 v26, v26
v_exp_f32 v27, v27
v_exp_f32 v28, v28
v_smfmac_f32_32x32x32_bf16 v[96:111], v[198:201], v[16:23], v14
v_exp_f32 v29, v29
v_exp_f32 v30, v30
v_exp_f32 v31, v31
v_exp_f32 v32, v32
v_exp_f32 v33, v33
v_smfmac_f32_32x32x32_bf16 v[96:111], v[202:205], v[24:31], v14
buffer_load_dwordx4 a[8:11], v2, s[12:15], s64 offen offset:2048 sc1
v_exp_f32 v34, v34
v_exp_f32 v35, v35
v_exp_f32 v36, v36
v_mfma_f32_32x32x16_bf16 v[48:63], v[170:173], v[152:155], v[48:63]
v_exp_f32 v37, v37
v_exp_f32 v38, v38
v_exp_f32 v39, v39
v_mfma_f32_32x32x16_bf16 v[64:79], v[170:173], v[156:159], v[64:79]
buffer_load_dwordx4 a[12:15], v2, s[12:15], s64 offen offset:3072 sc1
s_add_u32 s64, s64, 4096
v_exp_f32 v40, v40
v_exp_f32 v41, v41
v_exp_f32 v42, v42
v_smfmac_f32_32x32x32_bf16 v[112:127], v[198:201], v[32:39], v14
v_exp_f32 v43, v43
v_exp_f32 v44, v44
v_exp_f32 v45, v45
v_exp_f32 v46, v46
v_exp_f32 v47, v47
s_waitcnt vmcnt(13)
v_exp_f32 v48, v48
v_exp_f32 v49, v49
v_smfmac_f32_32x32x32_bf16 v[112:127], v[202:205], v[40:47], v14
buffer_load_dwordx4 v[170:173], v8, s[12:15], s83 offen sc1
v_exp_f32 v50, v50
v_exp_f32 v51, v51
v_exp_f32 v52, v52
v_mfma_f32_32x32x4_2b_bf16 v[16:47], v[180:181], v[160:161], 0
buffer_load_dwordx2 v[174:175], v9, s[12:15], s83 offen sc1
v_exp_f32 v53, v53
v_exp_f32 v54, v54
v_exp_f32 v55, v55
v_exp_f32 v56, v56
v_exp_f32 v57, v57
v_exp_f32 v58, v58
v_exp_f32 v59, v59
v_exp_f32 v60, v60
v_smfmac_f32_32x32x32_bf16 v[128:143], v[198:201], v[48:55], v14
v_exp_f32 v61, v61
v_exp_f32 v62, v62
v_exp_f32 v63, v63
v_exp_f32 v64, v64
v_exp_f32 v65, v65
v_smfmac_f32_32x32x32_bf16 v[128:143], v[202:205], v[56:63], v14
buffer_load_dwordx4 a[16:19], v2, s[12:15], s64 offen offset:0 sc1
v_exp_f32 v66, v66
v_exp_f32 v67, v67
v_exp_f32 v68, v68
v_mfma_f32_32x32x16_bf16 v[16:31], v[176:179], v[144:147], v[16:31]
v_exp_f32 v69, v69
v_exp_f32 v70, v70
v_exp_f32 v71, v71
v_mfma_f32_32x32x16_bf16 v[32:47], v[176:179], v[148:151], v[32:47]
buffer_load_dwordx4 a[20:23], v2, s[12:15], s64 offen offset:1024 sc1
v_exp_f32 v72, v72
v_exp_f32 v73, v73
v_exp_f32 v74, v74
v_smfmac_f32_32x32x32_bf16 v[80:95], v[198:201], v[64:71], v14
v_exp_f32 v75, v75
v_exp_f32 v76, v76
v_exp_f32 v77, v77
v_exp_f32 v78, v78
v_exp_f32 v79, v79
v_exp_f32 v16, v16
v_exp_f32 v17, v17
v_smfmac_f32_32x32x32_bf16 v[80:95], v[202:205], v[72:79], v14
buffer_load_dwordx4 v[198:201], v10, s[12:15], s84 offen sc1
v_exp_f32 v18, v18
v_exp_f32 v19, v19
v_exp_f32 v20, v20
v_mfma_f32_32x32x4_2b_bf16 v[48:79], v[180:181], v[162:163], 0
buffer_load_dwordx4 v[202:205], v11, s[12:15], s84 offen sc1
v_exp_f32 v21, v21
v_exp_f32 v22, v22
v_exp_f32 v23, v23
v_exp_f32 v24, v24
v_exp_f32 v25, v25
v_exp_f32 v26, v26
v_exp_f32 v27, v27
v_exp_f32 v28, v28
v_smfmac_f32_32x32x32_bf16 v[96:111], v[206:209], v[16:23], v14
v_exp_f32 v29, v29
v_exp_f32 v30, v30
v_exp_f32 v31, v31
v_exp_f32 v32, v32
v_exp_f32 v33, v33
v_smfmac_f32_32x32x32_bf16 v[96:111], v[210:213], v[24:31], v14
buffer_load_dwordx4 a[24:27], v2, s[12:15], s64 offen offset:2048 sc1
v_exp_f32 v34, v34
v_exp_f32 v35, v35
v_exp_f32 v36, v36
v_mfma_f32_32x32x16_bf16 v[48:63], v[176:179], v[152:155], v[48:63]
v_exp_f32 v37, v37
v_exp_f32 v38, v38
v_exp_f32 v39, v39
v_mfma_f32_32x32x16_bf16 v[64:79], v[176:179], v[156:159], v[64:79]
buffer_load_dwordx4 a[28:31], v2, s[12:15], s64 offen offset:3072 sc1
s_add_u32 s64, s64, 4096
v_exp_f32 v40, v40
v_exp_f32 v41, v41
v_exp_f32 v42, v42
v_smfmac_f32_32x32x32_bf16 v[112:127], v[206:209], v[32:39], v14
v_exp_f32 v43, v43
v_exp_f32 v44, v44
v_exp_f32 v45, v45
v_exp_f32 v46, v46
v_exp_f32 v47, v47
s_waitcnt vmcnt(14)
v_exp_f32 v48, v48
v_exp_f32 v49, v49
v_smfmac_f32_32x32x32_bf16 v[112:127], v[210:213], v[40:47], v14
buffer_load_dwordx4 v[176:179], v8, s[12:15], s84 offen sc1
v_exp_f32 v50, v50
v_exp_f32 v51, v51
v_exp_f32 v52, v52
v_mfma_f32_32x32x4_2b_bf16 v[16:47], v[168:169], v[160:161], 0
buffer_load_dwordx2 v[180:181], v9, s[12:15], s84 offen sc1
v_exp_f32 v53, v53
v_exp_f32 v54, v54
v_exp_f32 v55, v55
v_exp_f32 v56, v56
v_exp_f32 v57, v57
v_exp_f32 v58, v58
v_exp_f32 v59, v59
v_exp_f32 v60, v60
v_smfmac_f32_32x32x32_bf16 v[128:143], v[206:209], v[48:55], v14
v_exp_f32 v61, v61
v_exp_f32 v62, v62
v_exp_f32 v63, v63
v_exp_f32 v64, v64
v_exp_f32 v65, v65
v_smfmac_f32_32x32x32_bf16 v[128:143], v[210:213], v[56:63], v14
buffer_load_dwordx4 a[32:35], v2, s[12:15], s64 offen offset:0 sc1
v_exp_f32 v66, v66
v_exp_f32 v67, v67
v_exp_f32 v68, v68
v_mfma_f32_32x32x16_bf16 v[16:31], v[164:167], v[144:147], v[16:31]
v_exp_f32 v69, v69
v_exp_f32 v70, v70
v_exp_f32 v71, v71
v_mfma_f32_32x32x16_bf16 v[32:47], v[164:167], v[148:151], v[32:47]
buffer_load_dwordx4 a[36:39], v2, s[12:15], s64 offen offset:1024 sc1
v_exp_f32 v72, v72
v_exp_f32 v73, v73
v_exp_f32 v74, v74
v_smfmac_f32_32x32x32_bf16 v[80:95], v[206:209], v[64:71], v14
v_exp_f32 v75, v75
v_exp_f32 v76, v76
v_exp_f32 v77, v77
v_exp_f32 v78, v78
v_exp_f32 v79, v79
v_exp_f32 v16, v16
v_exp_f32 v17, v17
v_smfmac_f32_32x32x32_bf16 v[80:95], v[210:213], v[72:79], v14
buffer_load_dwordx4 v[206:209], v10, s[12:15], s85 offen sc1
v_exp_f32 v18, v18
v_exp_f32 v19, v19
v_exp_f32 v20, v20
v_mfma_f32_32x32x4_2b_bf16 v[48:79], v[168:169], v[162:163], 0
buffer_load_dwordx4 v[210:213], v11, s[12:15], s85 offen sc1
v_exp_f32 v21, v21
v_exp_f32 v22, v22
v_exp_f32 v23, v23
v_exp_f32 v24, v24
v_exp_f32 v25, v25
v_exp_f32 v26, v26
v_exp_f32 v27, v27
v_exp_f32 v28, v28
v_smfmac_f32_32x32x32_bf16 v[96:111], v[182:185], v[16:23], v14
v_exp_f32 v29, v29
v_exp_f32 v30, v30
v_exp_f32 v31, v31
v_exp_f32 v32, v32
v_exp_f32 v33, v33
v_smfmac_f32_32x32x32_bf16 v[96:111], v[186:189], v[24:31], v14
buffer_load_dwordx4 a[40:43], v2, s[12:15], s64 offen offset:2048 sc1
v_exp_f32 v34, v34
v_exp_f32 v35, v35
v_exp_f32 v36, v36
v_mfma_f32_32x32x16_bf16 v[48:63], v[164:167], v[152:155], v[48:63]
v_exp_f32 v37, v37
v_exp_f32 v38, v38
v_exp_f32 v39, v39
v_mfma_f32_32x32x16_bf16 v[64:79], v[164:167], v[156:159], v[64:79]
buffer_load_dwordx4 a[44:47], v2, s[12:15], s64 offen offset:3072 sc1
s_add_u32 s64, s64, 4096
v_exp_f32 v40, v40
v_exp_f32 v41, v41
v_exp_f32 v42, v42
v_smfmac_f32_32x32x32_bf16 v[112:127], v[182:185], v[32:39], v14
v_exp_f32 v43, v43
v_exp_f32 v44, v44
v_exp_f32 v45, v45
v_exp_f32 v46, v46
v_exp_f32 v47, v47
s_waitcnt vmcnt(14)
v_exp_f32 v48, v48
v_exp_f32 v49, v49
v_smfmac_f32_32x32x32_bf16 v[112:127], v[186:189], v[40:47], v14
buffer_load_dwordx4 v[164:167], v8, s[12:15], s85 offen sc1
v_exp_f32 v50, v50
v_exp_f32 v51, v51
v_exp_f32 v52, v52
v_mfma_f32_32x32x4_2b_bf16 v[16:47], v[174:175], v[160:161], 0
buffer_load_dwordx2 v[168:169], v9, s[12:15], s85 offen sc1
v_exp_f32 v53, v53
v_exp_f32 v54, v54
v_exp_f32 v55, v55
v_exp_f32 v56, v56
v_exp_f32 v57, v57
v_exp_f32 v58, v58
v_exp_f32 v59, v59
v_exp_f32 v60, v60
v_smfmac_f32_32x32x32_bf16 v[128:143], v[182:185], v[48:55], v14
v_exp_f32 v61, v61
v_exp_f32 v62, v62
v_exp_f32 v63, v63
v_exp_f32 v64, v64
v_exp_f32 v65, v65
v_smfmac_f32_32x32x32_bf16 v[128:143], v[186:189], v[56:63], v14
buffer_load_dwordx4 a[48:51], v2, s[12:15], s64 offen offset:0 sc1
v_exp_f32 v66, v66
v_exp_f32 v67, v67
v_exp_f32 v68, v68
v_mfma_f32_32x32x16_bf16 v[16:31], v[170:173], v[144:147], v[16:31]
v_exp_f32 v69, v69
v_exp_f32 v70, v70
v_exp_f32 v71, v71
v_mfma_f32_32x32x16_bf16 v[32:47], v[170:173], v[148:151], v[32:47]
buffer_load_dwordx4 a[52:55], v2, s[12:15], s64 offen offset:1024 sc1
v_exp_f32 v72, v72
v_exp_f32 v73, v73
v_exp_f32 v74, v74
v_smfmac_f32_32x32x32_bf16 v[80:95], v[182:185], v[64:71], v14
v_exp_f32 v75, v75
v_exp_f32 v76, v76
v_exp_f32 v77, v77
v_exp_f32 v78, v78
v_exp_f32 v79, v79
v_exp_f32 v16, v16
v_exp_f32 v17, v17
v_smfmac_f32_32x32x32_bf16 v[80:95], v[186:189], v[72:79], v14
buffer_load_dwordx4 v[182:185], v10, s[12:15], s70 offen sc1
v_exp_f32 v18, v18
v_exp_f32 v19, v19
v_exp_f32 v20, v20
v_mfma_f32_32x32x4_2b_bf16 v[48:79], v[174:175], v[162:163], 0
buffer_load_dwordx4 v[186:189], v11, s[12:15], s70 offen sc1
v_exp_f32 v21, v21
v_exp_f32 v22, v22
v_exp_f32 v23, v23
v_exp_f32 v24, v24
v_exp_f32 v25, v25
v_exp_f32 v26, v26
v_exp_f32 v27, v27
v_exp_f32 v28, v28
v_smfmac_f32_32x32x32_bf16 v[96:111], v[190:193], v[16:23], v14
v_exp_f32 v29, v29
v_exp_f32 v30, v30
v_exp_f32 v31, v31
v_exp_f32 v32, v32
v_exp_f32 v33, v33
v_smfmac_f32_32x32x32_bf16 v[96:111], v[194:197], v[24:31], v14
buffer_load_dwordx4 a[56:59], v2, s[12:15], s64 offen offset:2048 sc1
v_exp_f32 v34, v34
v_exp_f32 v35, v35
v_exp_f32 v36, v36
v_mfma_f32_32x32x16_bf16 v[48:63], v[170:173], v[152:155], v[48:63]
v_exp_f32 v37, v37
v_exp_f32 v38, v38
v_exp_f32 v39, v39
v_mfma_f32_32x32x16_bf16 v[64:79], v[170:173], v[156:159], v[64:79]
buffer_load_dwordx4 a[60:63], v2, s[12:15], s64 offen offset:3072 sc1
s_add_u32 s64, s64, 4096
v_exp_f32 v40, v40
v_exp_f32 v41, v41
v_exp_f32 v42, v42
v_smfmac_f32_32x32x32_bf16 v[112:127], v[190:193], v[32:39], v14
v_exp_f32 v43, v43
v_exp_f32 v44, v44
v_exp_f32 v45, v45
v_exp_f32 v46, v46
v_exp_f32 v47, v47
s_waitcnt vmcnt(14)
v_exp_f32 v48, v48
v_exp_f32 v49, v49
v_smfmac_f32_32x32x32_bf16 v[112:127], v[194:197], v[40:47], v14
buffer_load_dwordx4 v[170:173], v8, s[12:15], s70 offen sc1
v_exp_f32 v50, v50
v_exp_f32 v51, v51
v_exp_f32 v52, v52
v_mfma_f32_32x32x4_2b_bf16 v[16:47], v[180:181], v[160:161], 0
buffer_load_dwordx2 v[174:175], v9, s[12:15], s70 offen sc1
v_exp_f32 v53, v53
v_exp_f32 v54, v54
v_exp_f32 v55, v55
v_exp_f32 v56, v56
v_exp_f32 v57, v57
v_exp_f32 v58, v58
v_exp_f32 v59, v59
v_exp_f32 v60, v60
v_smfmac_f32_32x32x32_bf16 v[128:143], v[190:193], v[48:55], v14
v_exp_f32 v61, v61
v_exp_f32 v62, v62
v_exp_f32 v63, v63
v_exp_f32 v64, v64
v_exp_f32 v65, v65
v_smfmac_f32_32x32x32_bf16 v[128:143], v[194:197], v[56:63], v14
buffer_load_dwordx4 a[64:67], v2, s[12:15], s64 offen offset:0 sc1
v_exp_f32 v66, v66
v_exp_f32 v67, v67
v_exp_f32 v68, v68
v_mfma_f32_32x32x16_bf16 v[16:31], v[176:179], v[144:147], v[16:31]
v_exp_f32 v69, v69
v_exp_f32 v70, v70
v_exp_f32 v71, v71
v_mfma_f32_32x32x16_bf16 v[32:47], v[176:179], v[148:151], v[32:47]
v_exp_f32 v72, v72
v_exp_f32 v73, v73
v_exp_f32 v74, v74
v_smfmac_f32_32x32x32_bf16 v[80:95], v[190:193], v[64:71], v14
v_exp_f32 v75, v75
v_exp_f32 v76, v76
v_exp_f32 v77, v77
v_exp_f32 v78, v78
v_exp_f32 v79, v79
v_exp_f32 v16, v16
v_exp_f32 v17, v17
v_smfmac_f32_32x32x32_bf16 v[80:95], v[194:197], v[72:79], v14
buffer_load_dwordx4 v[190:193], v10, s[12:15], s71 offen sc1
v_exp_f32 v18, v18
v_exp_f32 v19, v19
v_exp_f32 v20, v20
v_mfma_f32_32x32x4_2b_bf16 v[48:79], v[180:181], v[162:163], 0
buffer_load_dwordx4 v[194:197], v11, s[12:15], s71 offen sc1
v_exp_f32 v21, v21
v_exp_f32 v22, v22
v_exp_f32 v23, v23
v_exp_f32 v24, v24
v_exp_f32 v25, v25
v_exp_f32 v26, v26
v_exp_f32 v27, v27
v_exp_f32 v28, v28
v_smfmac_f32_32x32x32_bf16 v[96:111], v[198:201], v[16:23], v14
v_exp_f32 v29, v29
v_exp_f32 v30, v30
v_exp_f32 v31, v31
v_exp_f32 v32, v32
)ASM"
      R"ASM(
v_exp_f32 v33, v33
v_smfmac_f32_32x32x32_bf16 v[96:111], v[202:205], v[24:31], v14
buffer_load_dwordx4 a[68:71], v2, s[12:15], s64 offen offset:1024 sc1
v_exp_f32 v34, v34
v_exp_f32 v35, v35
v_exp_f32 v36, v36
v_mfma_f32_32x32x16_bf16 v[48:63], v[176:179], v[152:155], v[48:63]
v_exp_f32 v37, v37
v_exp_f32 v38, v38
v_exp_f32 v39, v39
v_mfma_f32_32x32x16_bf16 v[64:79], v[176:179], v[156:159], v[64:79]
buffer_load_dwordx4 a[72:75], v2, s[12:15], s64 offen offset:2048 sc1
v_exp_f32 v40, v40
v_exp_f32 v41, v41
v_exp_f32 v42, v42
v_smfmac_f32_32x32x32_bf16 v[112:127], v[198:201], v[32:39], v14
v_exp_f32 v43, v43
v_exp_f32 v44, v44
v_exp_f32 v45, v45
v_exp_f32 v46, v46
v_exp_f32 v47, v47
s_waitcnt vmcnt(13)
v_exp_f32 v48, v48
v_exp_f32 v49, v49
v_smfmac_f32_32x32x32_bf16 v[112:127], v[202:205], v[40:47], v14
buffer_load_dwordx4 v[176:179], v8, s[12:15], s71 offen sc1
v_exp_f32 v50, v50
v_exp_f32 v51, v51
v_exp_f32 v52, v52
v_mfma_f32_32x32x4_2b_bf16 v[16:47], v[168:169], v[160:161], 0
buffer_load_dwordx2 v[180:181], v9, s[12:15], s71 offen sc1
v_exp_f32 v53, v53
v_exp_f32 v54, v54
v_exp_f32 v55, v55
v_exp_f32 v56, v56
v_exp_f32 v57, v57
v_exp_f32 v58, v58
v_exp_f32 v59, v59
v_exp_f32 v60, v60
v_smfmac_f32_32x32x32_bf16 v[128:143], v[198:201], v[48:55], v14
v_exp_f32 v61, v61
v_exp_f32 v62, v62
v_exp_f32 v63, v63
v_exp_f32 v64, v64
v_exp_f32 v65, v65
v_smfmac_f32_32x32x32_bf16 v[128:143], v[202:205], v[56:63], v14
buffer_load_dwordx4 a[76:79], v2, s[12:15], s64 offen offset:3072 sc1
s_add_u32 s64, s64, 4096
v_exp_f32 v66, v66
v_exp_f32 v67, v67
v_exp_f32 v68, v68
v_mfma_f32_32x32x16_bf16 v[16:31], v[164:167], v[144:147], v[16:31]
v_exp_f32 v69, v69
v_exp_f32 v70, v70
v_exp_f32 v71, v71
v_mfma_f32_32x32x16_bf16 v[32:47], v[164:167], v[148:151], v[32:47]
buffer_load_dwordx4 a[80:83], v2, s[12:15], s64 offen offset:0 sc1
v_exp_f32 v72, v72
v_exp_f32 v73, v73
v_exp_f32 v74, v74
v_smfmac_f32_32x32x32_bf16 v[80:95], v[198:201], v[64:71], v14
v_exp_f32 v75, v75
v_exp_f32 v76, v76
v_exp_f32 v77, v77
v_exp_f32 v78, v78
v_exp_f32 v79, v79
v_exp_f32 v16, v16
v_exp_f32 v17, v17
v_smfmac_f32_32x32x32_bf16 v[80:95], v[202:205], v[72:79], v14
buffer_load_dwordx4 v[198:201], v10, s[12:15], s72 offen sc1
v_exp_f32 v18, v18
v_exp_f32 v19, v19
v_exp_f32 v20, v20
v_mfma_f32_32x32x4_2b_bf16 v[48:79], v[168:169], v[162:163], 0
buffer_load_dwordx4 v[202:205], v11, s[12:15], s72 offen sc1
v_exp_f32 v21, v21
v_exp_f32 v22, v22
v_exp_f32 v23, v23
v_exp_f32 v24, v24
v_exp_f32 v25, v25
v_exp_f32 v26, v26
v_exp_f32 v27, v27
v_exp_f32 v28, v28
v_smfmac_f32_32x32x32_bf16 v[96:111], v[206:209], v[16:23], v14
s_add_u32 s60, s60, 163840
s_add_u32 s61, s61, 163840
s_add_u32 s62, s62, 163840
s_add_u32 s63, s63, 163840
s_add_u32 s88, s88, 163840
v_exp_f32 v29, v29
v_exp_f32 v30, v30
v_exp_f32 v31, v31
v_exp_f32 v32, v32
v_exp_f32 v33, v33
v_smfmac_f32_32x32x32_bf16 v[96:111], v[210:213], v[24:31], v14
buffer_load_dwordx4 a[84:87], v2, s[12:15], s64 offen offset:1024 sc1
v_exp_f32 v34, v34
v_exp_f32 v35, v35
v_exp_f32 v36, v36
v_mfma_f32_32x32x16_bf16 v[48:63], v[164:167], v[152:155], v[48:63]
buffer_load_dwordx4 v[144:147], v8, s[12:15], s60 offen sc1
v_exp_f32 v37, v37
v_exp_f32 v38, v38
v_exp_f32 v39, v39
v_mfma_f32_32x32x16_bf16 v[64:79], v[164:167], v[156:159], v[64:79]
buffer_load_dwordx4 a[88:91], v2, s[12:15], s64 offen offset:2048 sc1
v_exp_f32 v40, v40
v_exp_f32 v41, v41
v_exp_f32 v42, v42
v_smfmac_f32_32x32x32_bf16 v[112:127], v[206:209], v[32:39], v14
buffer_load_dwordx4 v[148:151], v8, s[12:15], s61 offen sc1
buffer_load_dwordx2 v[160:161], v12, s[12:15], s88 offen sc1
v_exp_f32 v43, v43
v_exp_f32 v44, v44
v_exp_f32 v45, v45
v_exp_f32 v46, v46
v_exp_f32 v47, v47
v_exp_f32 v48, v48
v_exp_f32 v49, v49
v_smfmac_f32_32x32x32_bf16 v[112:127], v[210:213], v[40:47], v14
s_add_u32 s50, s47, 1
s_and_b32 s50, s50, 3
s_mul_i32 s50, s50, 9216
s_add_u32 s50, s50, 102400
v_add_u32 v15, s50, v2
buffer_load_dwordx4 v[164:167], v8, s[12:15], s72 offen sc1
ds_write_b128 v15, v[96:99] offset:0
ds_write_b128 v15, v[100:103] offset:1024
ds_write_b128 v15, v[104:107] offset:2048
v_exp_f32 v50, v50
v_exp_f32 v51, v51
v_exp_f32 v52, v52
buffer_load_dwordx2 v[168:169], v9, s[12:15], s72 offen sc1
v_exp_f32 v53, v53
v_exp_f32 v54, v54
v_exp_f32 v55, v55
v_exp_f32 v56, v56
v_exp_f32 v57, v57
v_exp_f32 v58, v58
v_exp_f32 v59, v59
v_exp_f32 v60, v60
v_smfmac_f32_32x32x32_bf16 v[128:143], v[206:209], v[48:55], v14
buffer_load_dwordx4 v[152:155], v8, s[12:15], s62 offen sc1
s_add_u32 s50, s47, 2
s_and_b32 s50, s50, 3
s_mul_i32 s50, s50, 9216
s_add_u32 s50, s50, 102400
v_add_u32 v15, s50, v2
v_exp_f32 v61, v61
v_exp_f32 v62, v62
v_exp_f32 v63, v63
v_exp_f32 v64, v64
v_exp_f32 v65, v65
v_smfmac_f32_32x32x32_bf16 v[128:143], v[210:213], v[56:63], v14
buffer_load_dwordx4 a[92:95], v2, s[12:15], s64 offen offset:3072 sc1
s_add_u32 s64, s64, 4096
buffer_load_dwordx4 v[156:159], v8, s[12:15], s63 offen sc1
ds_write_b128 v15, v[112:115] offset:3072
ds_write_b128 v15, v[116:119] offset:4096
ds_write_b128 v15, v[120:123] offset:5120
v_exp_f32 v66, v66
v_exp_f32 v67, v67
v_exp_f32 v68, v68
v_exp_f32 v69, v69
v_exp_f32 v70, v70
v_exp_f32 v71, v71
buffer_load_dwordx4 a[96:99], v2, s[12:15], s64 offen offset:0 sc1
v_exp_f32 v72, v72
v_exp_f32 v73, v73
v_exp_f32 v74, v74
v_smfmac_f32_32x32x32_bf16 v[80:95], v[206:209], v[64:71], v14
buffer_load_dwordx2 v[162:163], v13, s[12:15], s88 offen sc1
v_exp_f32 v75, v75
v_exp_f32 v76, v76
v_exp_f32 v77, v77
v_exp_f32 v78, v78
v_exp_f32 v79, v79
s_add_u32 s50, s47, 3
s_and_b32 s50, s50, 3
s_mul_i32 s50, s50, 9216
s_add_u32 s50, s50, 102400
v_add_u32 v15, s50, v2
v_smfmac_f32_32x32x32_bf16 v[80:95], v[210:213], v[72:79], v14
ds_write_b128 v15, v[128:131] offset:6144
ds_write_b128 v15, v[132:135] offset:7168
ds_write_b128 v15, v[136:139] offset:8192
s_mul_i32 s50, s47, 9216
s_add_u32 s50, s50, 102400
v_add_u32 v15, s50, v2
s_waitcnt lgkmcnt(0)
s_barrier
ds_read_b128 v[16:19], v15 offset:0
ds_read_b128 v[20:23], v15 offset:1024
ds_read_b128 v[24:27], v15 offset:2048
ds_read_b128 v[28:31], v15 offset:3072
ds_read_b128 v[32:35], v15 offset:4096
ds_read_b128 v[36:39], v15 offset:5120
ds_read_b128 v[40:43], v15 offset:6144
ds_read_b128 v[44:47], v15 offset:7168
ds_read_b128 v[48:51], v15 offset:8192
s_waitcnt lgkmcnt(6)
v_pk_add_f32 v[80:81], v[80:81], v[16:17]
v_pk_add_f32 v[82:83], v[82:83], v[18:19]
v_pk_add_f32 v[84:85], v[84:85], v[20:21]
v_pk_add_f32 v[86:87], v[86:87], v[22:23]
v_pk_add_f32 v[88:89], v[88:89], v[24:25]
v_pk_add_f32 v[90:91], v[90:91], v[26:27]
s_waitcnt lgkmcnt(3)
v_pk_add_f32 v[80:81], v[80:81], v[28:29]
v_pk_add_f32 v[82:83], v[82:83], v[30:31]
v_pk_add_f32 v[84:85], v[84:85], v[32:33]
v_pk_add_f32 v[86:87], v[86:87], v[34:35]
v_pk_add_f32 v[88:89], v[88:89], v[36:37]
v_pk_add_f32 v[90:91], v[90:91], v[38:39]
s_waitcnt lgkmcnt(0)
v_pk_add_f32 v[80:81], v[80:81], v[40:41]
v_pk_add_f32 v[82:83], v[82:83], v[42:43]
v_pk_add_f32 v[84:85], v[84:85], v[44:45]
v_pk_add_f32 v[86:87], v[86:87], v[46:47]
v_pk_add_f32 v[88:89], v[88:89], v[48:49]
v_pk_add_f32 v[90:91], v[90:91], v[50:51]
v_mov_b32 v53, v88
v_mov_b32 v52, v88
s_nop 1
v_permlane32_swap_b32 v53, v52
v_pk_mul_f32 v[54:55], v[80:81], 0
v_pk_fma_f32 v[54:55], v[82:83], 0, v[54:55]
v_pk_fma_f32 v[54:55], v[84:85], 0, v[54:55]
v_pk_fma_f32 v[54:55], v[86:87], 0, v[54:55]
v_pk_fma_f32 v[54:55], v[88:89], 0, v[54:55]
v_pk_fma_f32 v[54:55], v[90:91], 0, v[54:55]
v_cmp_nle_f32 vcc, 0x21800000, v52
v_add_f32 v54, v54, v55
v_mov_b32 v53, 0x71800000
v_cmp_nle_f32 s[50:51], v52, v53
v_cmp_u_f32 s[52:53], v54, v54
s_or_b64 vcc, vcc, s[50:51]
s_or_b64 vcc, vcc, s[52:53]
s_cbranch_vccz .Lmk_ok_30
s_sub_u32 s50, s63, 163840
v_lshrrev_b32 v60, 5, v0
v_lshl_add_u32 v60, v60, 30, v9
buffer_load_dwordx4 v[32:35], v8, s[12:15], s50 offen sc1
buffer_load_dwordx2 v[36:37], v60, s[12:15], s50 offen sc1
v_mov_b32 v80, 0
v_mov_b32 v81, 0
v_mov_b32 v82, 0
v_mov_b32 v83, 0
v_mov_b32 v84, 0
v_mov_b32 v85, 0
v_mov_b32 v86, 0
v_mov_b32 v87, 0
v_mov_b32 v88, 0
v_mov_b32 v89, 0
v_mov_b32 v90, 0
v_mov_b32 v91, 0
v_mov_b32 v92, 0
v_mov_b32 v93, 0
v_mov_b32 v94, 0
v_mov_b32 v95, 0
v_mov_b32 v57, 0xff800000
s_mov_b32 s50, 4718592
s_mov_b32 s51, 64
.Lmk_fb_31:
buffer_load_dwordx4 v[38:41], v8, s[12:15], s50 offen sc1
buffer_load_dwordx2 v[42:43], v9, s[12:15], s50 offen sc1
buffer_load_dwordx4 v[48:51], v10, s[12:15], s50 offen sc1
buffer_load_dwordx4 v[52:55], v11, s[12:15], s50 offen sc1
s_waitcnt vmcnt(0)
v_mfma_f32_32x32x16_bf16 v[16:31], v[38:41], v[32:35], 0
v_mfma_f32_32x32x8_bf16 v[16:31], v[42:43], v[36:37], v[16:31]
s_nop 11
v_max3_f32 v56, v16, v17, v57
v_max3_f32 v56, v18, v19, v56
v_max3_f32 v56, v20, v21, v56
v_max3_f32 v56, v22, v23, v56
v_max3_f32 v56, v24, v25, v56
v_max3_f32 v56, v26, v27, v56
v_max3_f32 v56, v28, v29, v56
v_max3_f32 v56, v30, v31, v56
v_mov_b32 v58, v56
s_nop 1
v_permlane32_swap_b32 v56, v58
v_max_f32 v56, v56, v58
v_sub_f32 v59, v57, v56
v_exp_f32 v59, v59
v_mov_b32 v57, v56
v_sub_f32 v16, v16, v56
v_sub_f32 v17, v17, v56
v_sub_f32 v18, v18, v56
v_sub_f32 v19, v19, v56
v_sub_f32 v20, v20, v56
v_sub_f32 v21, v21, v56
v_sub_f32 v22, v22, v56
v_sub_f32 v23, v23, v56
v_sub_f32 v24, v24, v56
v_sub_f32 v25, v25, v56
v_sub_f32 v26, v26, v56
v_sub_f32 v27, v27, v56
v_sub_f32 v28, v28, v56
v_sub_f32 v29, v29, v56
v_sub_f32 v30, v30, v56
v_sub_f32 v31, v31, v56
v_exp_f32 v16, v16
v_exp_f32 v17, v17
v_exp_f32 v18, v18
v_exp_f32 v19, v19
v_exp_f32 v20, v20
v_exp_f32 v21, v21
v_exp_f32 v22, v22
v_exp_f32 v23, v23
v_exp_f32 v24, v24
v_exp_f32 v25, v25
v_exp_f32 v26, v26
v_exp_f32 v27, v27
v_exp_f32 v28, v28
v_exp_f32 v29, v29
v_exp_f32 v30, v30
v_exp_f32 v31, v31
v_mul_f32 v80, v80, v59
v_mul_f32 v81, v81, v59
v_mul_f32 v82, v82, v59
v_mul_f32 v83, v83, v59
v_mul_f32 v84, v84, v59
v_mul_f32 v85, v85, v59
v_mul_f32 v86, v86, v59
v_mul_f32 v87, v87, v59
v_mul_f32 v88, v88, v59
v_mul_f32 v89, v89, v59
v_mul_f32 v90, v90, v59
v_mul_f32 v91, v91, v59
s_nop 1
v_smfmac_f32_32x32x32_bf16 v[80:95], v[48:51], v[16:23], v14
v_smfmac_f32_32x32x32_bf16 v[80:95], v[52:55], v[24:31], v14
s_add_u32 s50, s50, 82048
s_sub_u32 s51, s51, 1
s_cmp_lg_u32 s51, 0
s_nop 11
s_cbranch_scc1 .Lmk_fb_31
v_mov_b32 v53, v88
v_mov_b32 v52, v88
s_nop 1
v_permlane32_swap_b32 v53, v52
.Lmk_ok_30:
s_waitcnt vmcnt(0)
v_mov_b32 v4, 0
v_mov_b32 v5, 0
v_mfma_f32_32x32x4_2b_bf16 v[16:47], v[174:175], v[160:161], 0
v_rcp_f32 v52, v52
s_nop 0
v_mul_f32 v80, v80, v52
v_mul_f32 v81, v81, v52
v_mul_f32 v82, v82, v52
v_mul_f32 v83, v83, v52
v_mul_f32 v84, v84, v52
v_mul_f32 v85, v85, v52
v_mul_f32 v86, v86, v52
v_mul_f32 v87, v87, v52
v_mul_f32 v88, v88, v52
v_mul_f32 v89, v89, v52
v_mul_f32 v90, v90, v52
v_mul_f32 v91, v91, v52
v_mfma_f32_32x32x8_bf16 v[96:111], v[4:5], v[4:5], 0
v_mfma_f32_32x32x8_bf16 v[112:127], v[4:5], v[4:5], 0
v_mfma_f32_32x32x8_bf16 v[128:143], v[4:5], v[4:5], 0
s_nop 0
v_mfma_f32_32x32x16_bf16 v[16:31], v[170:173], v[144:147], v[16:31]
v_mfma_f32_32x32x16_bf16 v[32:47], v[170:173], v[148:151], v[32:47]
v_cvt_pk_bf16_f32 v56, v80, v81
v_cvt_pk_bf16_f32 v57, v82, v83
v_cvt_pk_bf16_f32 v58, v84, v85
v_cvt_pk_bf16_f32 v59, v86, v87
v_cvt_pk_bf16_f32 v60, v88, v89
v_cvt_pk_bf16_f32 v61, v90, v91
buffer_store_dwordx4 v[56:59], v214, s[12:15], 0 offen sc1
buffer_store_dwordx2 v[60:61], v215, s[12:15], 0 offen sc1
v_mfma_f32_32x32x8_bf16 v[80:95], v[4:5], v[4:5], 0
s_nop 1
v_exp_f32 v16, v16
v_exp_f32 v17, v17
v_exp_f32 v18, v18
v_exp_f32 v19, v19
v_exp_f32 v20, v20
v_mfma_f32_32x32x4_2b_bf16 v[48:79], v[174:175], v[162:163], 0
buffer_load_dwordx4 v[206:209], v10, s[12:15], s73 offen sc1
v_exp_f32 v21, v21
v_exp_f32 v22, v22
v_exp_f32 v23, v23
v_exp_f32 v24, v24
v_exp_f32 v25, v25
v_exp_f32 v26, v26
v_exp_f32 v27, v27
v_exp_f32 v28, v28
v_smfmac_f32_32x32x32_bf16 v[96:111], v[182:185], v[16:23], v14
buffer_load_dwordx4 v[210:213], v11, s[12:15], s73 offen sc1
v_exp_f32 v29, v29
v_exp_f32 v30, v30
v_exp_f32 v31, v31
v_exp_f32 v32, v32
v_exp_f32 v33, v33
)ASM"
      R"ASM(
v_smfmac_f32_32x32x32_bf16 v[96:111], v[186:189], v[24:31], v14
buffer_load_dwordx4 a[100:103], v2, s[12:15], s64 offen offset:1024 sc1
v_exp_f32 v34, v34
v_exp_f32 v35, v35
v_exp_f32 v36, v36
v_mfma_f32_32x32x16_bf16 v[48:63], v[170:173], v[152:155], v[48:63]
v_exp_f32 v37, v37
v_exp_f32 v38, v38
v_exp_f32 v39, v39
v_mfma_f32_32x32x16_bf16 v[64:79], v[170:173], v[156:159], v[64:79]
buffer_load_dwordx4 a[104:107], v2, s[12:15], s64 offen offset:2048 sc1
v_exp_f32 v40, v40
v_exp_f32 v41, v41
v_exp_f32 v42, v42
v_smfmac_f32_32x32x32_bf16 v[112:127], v[182:185], v[32:39], v14
v_exp_f32 v43, v43
v_exp_f32 v44, v44
v_exp_f32 v45, v45
v_exp_f32 v46, v46
v_exp_f32 v47, v47
v_exp_f32 v48, v48
v_exp_f32 v49, v49
v_smfmac_f32_32x32x32_bf16 v[112:127], v[186:189], v[40:47], v14
buffer_load_dwordx4 v[170:173], v8, s[12:15], s73 offen sc1
v_exp_f32 v50, v50
v_exp_f32 v51, v51
v_exp_f32 v52, v52
v_mfma_f32_32x32x4_2b_bf16 v[16:47], v[180:181], v[160:161], 0
buffer_load_dwordx2 v[174:175], v9, s[12:15], s73 offen sc1
v_exp_f32 v53, v53
v_exp_f32 v54, v54
v_exp_f32 v55, v55
v_exp_f32 v56, v56
v_exp_f32 v57, v57
v_exp_f32 v58, v58
v_exp_f32 v59, v59
v_exp_f32 v60, v60
v_smfmac_f32_32x32x32_bf16 v[128:143], v[182:185], v[48:55], v14
v_exp_f32 v61, v61
v_exp_f32 v62, v62
v_exp_f32 v63, v63
v_exp_f32 v64, v64
v_exp_f32 v65, v65
v_smfmac_f32_32x32x32_bf16 v[128:143], v[186:189], v[56:63], v14
buffer_load_dwordx4 a[108:111], v2, s[12:15], s64 offen offset:3072 sc1
s_add_u32 s64, s64, 4096
v_exp_f32 v66, v66
v_exp_f32 v67, v67
v_exp_f32 v68, v68
v_mfma_f32_32x32x16_bf16 v[16:31], v[176:179], v[144:147], v[16:31]
v_exp_f32 v69, v69
v_exp_f32 v70, v70
v_exp_f32 v71, v71
v_mfma_f32_32x32x16_bf16 v[32:47], v[176:179], v[148:151], v[32:47]
buffer_load_dwordx4 a[112:115], v2, s[12:15], s64 offen offset:0 sc1
v_exp_f32 v72, v72
v_exp_f32 v73, v73
v_exp_f32 v74, v74
v_smfmac_f32_32x32x32_bf16 v[80:95], v[182:185], v[64:71], v14
v_exp_f32 v75, v75
v_exp_f32 v76, v76
v_exp_f32 v77, v77
v_exp_f32 v78, v78
v_exp_f32 v79, v79
v_exp_f32 v16, v16
v_exp_f32 v17, v17
v_smfmac_f32_32x32x32_bf16 v[80:95], v[186:189], v[72:79], v14
buffer_load_dwordx4 v[182:185], v10, s[12:15], s74 offen sc1
v_exp_f32 v18, v18
v_exp_f32 v19, v19
v_exp_f32 v20, v20
v_mfma_f32_32x32x4_2b_bf16 v[48:79], v[180:181], v[162:163], 0
buffer_load_dwordx4 v[186:189], v11, s[12:15], s74 offen sc1
v_exp_f32 v21, v21
v_exp_f32 v22, v22
v_exp_f32 v23, v23
v_exp_f32 v24, v24
v_exp_f32 v25, v25
v_exp_f32 v26, v26
v_exp_f32 v27, v27
v_exp_f32 v28, v28
v_smfmac_f32_32x32x32_bf16 v[96:111], v[190:193], v[16:23], v14
v_exp_f32 v29, v29
v_exp_f32 v30, v30
v_exp_f32 v31, v31
v_exp_f32 v32, v32
v_exp_f32 v33, v33
v_smfmac_f32_32x32x32_bf16 v[96:111], v[194:197], v[24:31], v14
buffer_load_dwordx4 a[116:119], v2, s[12:15], s64 offen offset:1024 sc1
v_exp_f32 v34, v34
v_exp_f32 v35, v35
v_exp_f32 v36, v36
v_mfma_f32_32x32x16_bf16 v[48:63], v[176:179], v[152:155], v[48:63]
v_exp_f32 v37, v37
v_exp_f32 v38, v38
v_exp_f32 v39, v39
v_mfma_f32_32x32x16_bf16 v[64:79], v[176:179], v[156:159], v[64:79]
buffer_load_dwordx4 a[120:123], v2, s[12:15], s64 offen offset:2048 sc1
v_exp_f32 v40, v40
v_exp_f32 v41, v41
v_exp_f32 v42, v42
v_smfmac_f32_32x32x32_bf16 v[112:127], v[190:193], v[32:39], v14
v_exp_f32 v43, v43
v_exp_f32 v44, v44
v_exp_f32 v45, v45
v_exp_f32 v46, v46
v_exp_f32 v47, v47
v_exp_f32 v48, v48
v_exp_f32 v49, v49
v_smfmac_f32_32x32x32_bf16 v[112:127], v[194:197], v[40:47], v14
buffer_load_dwordx4 v[176:179], v8, s[12:15], s74 offen sc1
v_exp_f32 v50, v50
v_exp_f32 v51, v51
v_exp_f32 v52, v52
v_mfma_f32_32x32x4_2b_bf16 v[16:47], v[168:169], v[160:161], 0
buffer_load_dwordx2 v[180:181], v9, s[12:15], s74 offen sc1
v_exp_f32 v53, v53
v_exp_f32 v54, v54
v_exp_f32 v55, v55
v_exp_f32 v56, v56
v_exp_f32 v57, v57
v_exp_f32 v58, v58
v_exp_f32 v59, v59
v_exp_f32 v60, v60
v_smfmac_f32_32x32x32_bf16 v[128:143], v[190:193], v[48:55], v14
v_exp_f32 v61, v61
v_exp_f32 v62, v62
v_exp_f32 v63, v63
v_exp_f32 v64, v64
v_exp_f32 v65, v65
v_smfmac_f32_32x32x32_bf16 v[128:143], v[194:197], v[56:63], v14
buffer_load_dwordx4 a[124:127], v2, s[12:15], s64 offen offset:3072 sc1
s_add_u32 s64, s64, 4096
v_exp_f32 v66, v66
v_exp_f32 v67, v67
v_exp_f32 v68, v68
v_mfma_f32_32x32x16_bf16 v[16:31], v[164:167], v[144:147], v[16:31]
v_exp_f32 v69, v69
v_exp_f32 v70, v70
v_exp_f32 v71, v71
v_mfma_f32_32x32x16_bf16 v[32:47], v[164:167], v[148:151], v[32:47]
buffer_load_dwordx4 a[128:131], v2, s[12:15], s64 offen offset:0 sc1
v_exp_f32 v72, v72
v_exp_f32 v73, v73
v_exp_f32 v74, v74
v_smfmac_f32_32x32x32_bf16 v[80:95], v[190:193], v[64:71], v14
v_exp_f32 v75, v75
v_exp_f32 v76, v76
v_exp_f32 v77, v77
v_exp_f32 v78, v78
v_exp_f32 v79, v79
s_waitcnt vmcnt(16)
v_mov_b32 v4, s44
v_mov_b32 v5, s45
s_mov_b64 exec, 0xffff
buffer_store_dwordx2 v[4:5], v3, s[12:15], s68 offen sc1
s_mov_b64 exec, -1
v_exp_f32 v16, v16
v_exp_f32 v17, v17
v_smfmac_f32_32x32x32_bf16 v[80:95], v[194:197], v[72:79], v14
buffer_load_dwordx4 v[190:193], v10, s[12:15], s75 offen sc1
v_exp_f32 v18, v18
v_exp_f32 v19, v19
v_exp_f32 v20, v20
v_mfma_f32_32x32x4_2b_bf16 v[48:79], v[168:169], v[162:163], 0
buffer_load_dwordx4 v[194:197], v11, s[12:15], s75 offen sc1
v_exp_f32 v21, v21
v_exp_f32 v22, v22
v_exp_f32 v23, v23
v_exp_f32 v24, v24
v_exp_f32 v25, v25
v_exp_f32 v26, v26
v_exp_f32 v27, v27
v_exp_f32 v28, v28
v_smfmac_f32_32x32x32_bf16 v[96:111], v[198:201], v[16:23], v14
v_exp_f32 v29, v29
v_exp_f32 v30, v30
v_exp_f32 v31, v31
v_exp_f32 v32, v32
v_exp_f32 v33, v33
v_smfmac_f32_32x32x32_bf16 v[96:111], v[202:205], v[24:31], v14
buffer_load_dwordx4 a[132:135], v2, s[12:15], s64 offen offset:1024 sc1
v_exp_f32 v34, v34
v_exp_f32 v35, v35
v_exp_f32 v36, v36
v_mfma_f32_32x32x16_bf16 v[48:63], v[164:167], v[152:155], v[48:63]
v_exp_f32 v37, v37
v_exp_f32 v38, v38
v_exp_f32 v39, v39
v_mfma_f32_32x32x16_bf16 v[64:79], v[164:167], v[156:159], v[64:79]
buffer_load_dwordx4 a[136:139], v2, s[12:15], s64 offen offset:2048 sc1
v_exp_f32 v40, v40
v_exp_f32 v41, v41
v_exp_f32 v42, v42
v_smfmac_f32_32x32x32_bf16 v[112:127], v[198:201], v[32:39], v14
v_exp_f32 v43, v43
v_exp_f32 v44, v44
v_exp_f32 v45, v45
v_exp_f32 v46, v46
v_exp_f32 v47, v47
s_waitcnt vmcnt(15)
v_exp_f32 v48, v48
v_exp_f32 v49, v49
v_smfmac_f32_32x32x32_bf16 v[112:127], v[202:205], v[40:47], v14
buffer_load_dwordx4 v[164:167], v8, s[12:15], s75 offen sc1
v_exp_f32 v50, v50
v_exp_f32 v51, v51
v_exp_f32 v52, v52
v_mfma_f32_32x32x4_2b_bf16 v[16:47], v[174:175], v[160:161], 0
buffer_load_dwordx2 v[168:169], v9, s[12:15], s75 offen sc1
v_exp_f32 v53, v53
v_exp_f32 v54, v54
v_exp_f32 v55, v55
v_exp_f32 v56, v56
v_exp_f32 v57, v57
v_exp_f32 v58, v58
v_exp_f32 v59, v59
v_exp_f32 v60, v60
v_smfmac_f32_32x32x32_bf16 v[128:143], v[198:201], v[48:55], v14
v_exp_f32 v61, v61
v_exp_f32 v62, v62
v_exp_f32 v63, v63
v_exp_f32 v64, v64
v_exp_f32 v65, v65
v_smfmac_f32_32x32x32_bf16 v[128:143], v[202:205], v[56:63], v14
buffer_load_dwordx4 a[140:143], v2, s[12:15], s64 offen offset:3072 sc1
s_add_u32 s64, s64, 4096
v_exp_f32 v66, v66
v_exp_f32 v67, v67
v_exp_f32 v68, v68
v_mfma_f32_32x32x16_bf16 v[16:31], v[170:173], v[144:147], v[16:31]
v_exp_f32 v69, v69
v_exp_f32 v70, v70
v_exp_f32 v71, v71
v_mfma_f32_32x32x16_bf16 v[32:47], v[170:173], v[148:151], v[32:47]
v_exp_f32 v72, v72
v_exp_f32 v73, v73
v_exp_f32 v74, v74
v_smfmac_f32_32x32x32_bf16 v[80:95], v[198:201], v[64:71], v14
v_exp_f32 v75, v75
v_exp_f32 v76, v76
v_exp_f32 v77, v77
v_exp_f32 v78, v78
v_exp_f32 v79, v79
v_exp_f32 v16, v16
v_exp_f32 v17, v17
v_smfmac_f32_32x32x32_bf16 v[80:95], v[202:205], v[72:79], v14
buffer_load_dwordx4 v[198:201], v10, s[12:15], s76 offen sc1
v_exp_f32 v18, v18
v_exp_f32 v19, v19
v_exp_f32 v20, v20
v_mfma_f32_32x32x4_2b_bf16 v[48:79], v[174:175], v[162:163], 0
buffer_load_dwordx4 v[202:205], v11, s[12:15], s76 offen sc1
v_exp_f32 v21, v21
v_exp_f32 v22, v22
v_exp_f32 v23, v23
v_exp_f32 v24, v24
v_exp_f32 v25, v25
v_exp_f32 v26, v26
v_exp_f32 v27, v27
v_exp_f32 v28, v28
v_smfmac_f32_32x32x32_bf16 v[96:111], v[206:209], v[16:23], v14
v_exp_f32 v29, v29
v_exp_f32 v30, v30
v_exp_f32 v31, v31
v_exp_f32 v32, v32
v_exp_f32 v33, v33
v_smfmac_f32_32x32x32_bf16 v[96:111], v[210:213], v[24:31], v14
buffer_load_dwordx4 a[144:147], v2, s[12:15], s64 offen offset:0 sc1
v_exp_f32 v34, v34
v_exp_f32 v35, v35
v_exp_f32 v36, v36
v_mfma_f32_32x32x16_bf16 v[48:63], v[170:173], v[152:155], v[48:63]
v_exp_f32 v37, v37
v_exp_f32 v38, v38
v_exp_f32 v39, v39
v_mfma_f32_32x32x16_bf16 v[64:79], v[170:173], v[156:159], v[64:79]
buffer_load_dwordx4 a[148:151], v2, s[12:15], s64 offen offset:1024 sc1
v_exp_f32 v40, v40
v_exp_f32 v41, v41
v_exp_f32 v42, v42
v_smfmac_f32_32x32x32_bf16 v[112:127], v[206:209], v[32:39], v14
v_exp_f32 v43, v43
v_exp_f32 v44, v44
v_exp_f32 v45, v45
v_exp_f32 v46, v46
v_exp_f32 v47, v47
s_waitcnt vmcnt(14)
v_exp_f32 v48, v48
v_exp_f32 v49, v49
v_smfmac_f32_32x32x32_bf16 v[112:127], v[210:213], v[40:47], v14
buffer_load_dwordx4 v[170:173], v8, s[12:15], s76 offen sc1
v_exp_f32 v50, v50
v_exp_f32 v51, v51
v_exp_f32 v52, v52
v_mfma_f32_32x32x4_2b_bf16 v[16:47], v[180:181], v[160:161], 0
buffer_load_dwordx2 v[174:175], v9, s[12:15], s76 offen sc1
v_exp_f32 v53, v53
v_exp_f32 v54, v54
v_exp_f32 v55, v55
v_exp_f32 v56, v56
v_exp_f32 v57, v57
v_exp_f32 v58, v58
v_exp_f32 v59, v59
v_exp_f32 v60, v60
v_smfmac_f32_32x32x32_bf16 v[128:143], v[206:209], v[48:55], v14
v_exp_f32 v61, v61
v_exp_f32 v62, v62
v_exp_f32 v63, v63
v_exp_f32 v64, v64
v_exp_f32 v65, v65
v_smfmac_f32_32x32x32_bf16 v[128:143], v[210:213], v[56:63], v14
buffer_load_dwordx4 a[152:155], v2, s[12:15], s64 offen offset:2048 sc1
v_exp_f32 v66, v66
v_exp_f32 v67, v67
v_exp_f32 v68, v68
v_mfma_f32_32x32x16_bf16 v[16:31], v[176:179], v[144:147], v[16:31]
v_exp_f32 v69, v69
v_exp_f32 v70, v70
v_exp_f32 v71, v71
v_mfma_f32_32x32x16_bf16 v[32:47], v[176:179], v[148:151], v[32:47]
buffer_load_dwordx4 a[156:159], v2, s[12:15], s64 offen offset:3072 sc1
s_add_u32 s64, s64, 4096
v_exp_f32 v72, v72
v_exp_f32 v73, v73
v_exp_f32 v74, v74
v_smfmac_f32_32x32x32_bf16 v[80:95], v[206:209], v[64:71], v14
v_exp_f32 v75, v75
v_exp_f32 v76, v76
v_exp_f32 v77, v77
v_exp_f32 v78, v78
v_exp_f32 v79, v79
s_barrier
v_exp_f32 v16, v16
v_exp_f32 v17, v17
v_smfmac_f32_32x32x32_bf16 v[80:95], v[210:213], v[72:79], v14
buffer_load_dwordx4 v[206:209], v10, s[12:15], s77 offen sc1
v_exp_f32 v18, v18
v_exp_f32 v19, v19
v_exp_f32 v20, v20
v_mfma_f32_32x32x4_2b_bf16 v[48:79], v[180:181], v[162:163], 0
buffer_load_dwordx4 v[210:213], v11, s[12:15], s77 offen sc1
v_exp_f32 v21, v21
v_exp_f32 v22, v22
v_exp_f32 v23, v23
v_exp_f32 v24, v24
v_exp_f32 v25, v25
v_exp_f32 v26, v26
v_exp_f32 v27, v27
v_exp_f32 v28, v28
v_smfmac_f32_32x32x32_bf16 v[96:111], v[182:185], v[16:23], v14
v_exp_f32 v29, v29
v_exp_f32 v30, v30
v_exp_f32 v31, v31
v_exp_f32 v32, v32
v_exp_f32 v33, v33
v_smfmac_f32_32x32x32_bf16 v[96:111], v[186:189], v[24:31], v14
buffer_load_dwordx4 a[160:163], v2, s[12:15], s65 offen offset:0 sc1
v_exp_f32 v34, v34
v_exp_f32 v35, v35
v_exp_f32 v36, v36
v_mfma_f32_32x32x16_bf16 v[48:63], v[176:179], v[152:155], v[48:63]
v_exp_f32 v37, v37
v_exp_f32 v38, v38
v_exp_f32 v39, v39
v_mfma_f32_32x32x16_bf16 v[64:79], v[176:179], v[156:159], v[64:79]
buffer_load_dwordx4 a[164:167], v2, s[12:15], s65 offen offset:1024 sc1
v_exp_f32 v40, v40
v_exp_f32 v41, v41
v_exp_f32 v42, v42
v_smfmac_f32_32x32x32_bf16 v[112:127], v[182:185], v[32:39], v14
v_exp_f32 v43, v43
v_exp_f32 v44, v44
v_exp_f32 v45, v45
v_exp_f32 v46, v46
v_exp_f32 v47, v47
s_waitcnt vmcnt(13)
v_exp_f32 v48, v48
v_exp_f32 v49, v49
v_smfmac_f32_32x32x32_bf16 v[112:127], v[186:189], v[40:47], v14
buffer_load_dwordx4 v[176:179], v8, s[12:15], s77 offen sc1
v_exp_f32 v50, v50
v_exp_f32 v51, v51
v_exp_f32 v52, v52
v_mfma_f32_32x32x4_2b_bf16 v[16:47], v[168:169], v[160:161], 0
buffer_load_dwordx2 v[180:181], v9, s[12:15], s77 offen sc1
v_exp_f32 v53, v53
v_exp_f32 v54, v54
v_exp_f32 v55, v55
v_exp_f32 v56, v56
v_exp_f32 v57, v57
v_exp_f32 v58, v58
v_exp_f32 v59, v59
v_exp_f32 v60, v60
v_smfmac_f32_32x32x32_bf16 v[128:143], v[182:185], v[48:55], v14
v_exp_f32 v61, v61
v_exp_f32 v62, v62
v_exp_f32 v63, v63
v_exp_f32 v64, v64
v_exp_f32 v65, v65
)ASM"
      R"ASM(
v_smfmac_f32_32x32x32_bf16 v[128:143], v[186:189], v[56:63], v14
buffer_load_dwordx4 a[168:171], v2, s[12:15], s65 offen offset:2048 sc1
v_exp_f32 v66, v66
v_exp_f32 v67, v67
v_exp_f32 v68, v68
v_mfma_f32_32x32x16_bf16 v[16:31], v[164:167], v[144:147], v[16:31]
v_exp_f32 v69, v69
v_exp_f32 v70, v70
v_exp_f32 v71, v71
v_mfma_f32_32x32x16_bf16 v[32:47], v[164:167], v[148:151], v[32:47]
buffer_load_dwordx4 a[172:175], v2, s[12:15], s65 offen offset:3072 sc1
s_add_u32 s65, s65, 4096
v_exp_f32 v72, v72
v_exp_f32 v73, v73
v_exp_f32 v74, v74
v_smfmac_f32_32x32x32_bf16 v[80:95], v[182:185], v[64:71], v14
v_exp_f32 v75, v75
v_exp_f32 v76, v76
v_exp_f32 v77, v77
v_exp_f32 v78, v78
v_exp_f32 v79, v79
v_exp_f32 v16, v16
v_exp_f32 v17, v17
v_smfmac_f32_32x32x32_bf16 v[80:95], v[186:189], v[72:79], v14
buffer_load_dwordx4 v[182:185], v10, s[12:15], s78 offen sc1
v_exp_f32 v18, v18
v_exp_f32 v19, v19
v_exp_f32 v20, v20
v_mfma_f32_32x32x4_2b_bf16 v[48:79], v[168:169], v[162:163], 0
buffer_load_dwordx4 v[186:189], v11, s[12:15], s78 offen sc1
v_exp_f32 v21, v21
v_exp_f32 v22, v22
v_exp_f32 v23, v23
v_exp_f32 v24, v24
v_exp_f32 v25, v25
v_exp_f32 v26, v26
v_exp_f32 v27, v27
v_exp_f32 v28, v28
v_smfmac_f32_32x32x32_bf16 v[96:111], v[190:193], v[16:23], v14
v_exp_f32 v29, v29
v_exp_f32 v30, v30
v_exp_f32 v31, v31
v_exp_f32 v32, v32
v_exp_f32 v33, v33
v_smfmac_f32_32x32x32_bf16 v[96:111], v[194:197], v[24:31], v14
buffer_load_dwordx4 a[176:179], v2, s[12:15], s65 offen offset:0 sc1
v_exp_f32 v34, v34
v_exp_f32 v35, v35
v_exp_f32 v36, v36
v_mfma_f32_32x32x16_bf16 v[48:63], v[164:167], v[152:155], v[48:63]
v_exp_f32 v37, v37
v_exp_f32 v38, v38
v_exp_f32 v39, v39
v_mfma_f32_32x32x16_bf16 v[64:79], v[164:167], v[156:159], v[64:79]
buffer_load_dwordx4 a[180:183], v2, s[12:15], s65 offen offset:1024 sc1
v_exp_f32 v40, v40
v_exp_f32 v41, v41
v_exp_f32 v42, v42
v_smfmac_f32_32x32x32_bf16 v[112:127], v[190:193], v[32:39], v14
v_exp_f32 v43, v43
v_exp_f32 v44, v44
v_exp_f32 v45, v45
v_exp_f32 v46, v46
v_exp_f32 v47, v47
s_waitcnt vmcnt(14)
v_exp_f32 v48, v48
v_exp_f32 v49, v49
v_smfmac_f32_32x32x32_bf16 v[112:127], v[194:197], v[40:47], v14
buffer_load_dwordx4 v[164:167], v8, s[12:15], s78 offen sc1
v_exp_f32 v50, v50
v_exp_f32 v51, v51
v_exp_f32 v52, v52
v_mfma_f32_32x32x4_2b_bf16 v[16:47], v[174:175], v[160:161], 0
buffer_load_dwordx2 v[168:169], v9, s[12:15], s78 offen sc1
v_exp_f32 v53, v53
v_exp_f32 v54, v54
v_exp_f32 v55, v55
v_exp_f32 v56, v56
v_exp_f32 v57, v57
v_exp_f32 v58, v58
v_exp_f32 v59, v59
v_exp_f32 v60, v60
v_smfmac_f32_32x32x32_bf16 v[128:143], v[190:193], v[48:55], v14
v_exp_f32 v61, v61
v_exp_f32 v62, v62
v_exp_f32 v63, v63
v_exp_f32 v64, v64
v_exp_f32 v65, v65
v_smfmac_f32_32x32x32_bf16 v[128:143], v[194:197], v[56:63], v14
buffer_load_dwordx4 a[184:187], v2, s[12:15], s65 offen offset:2048 sc1
v_exp_f32 v66, v66
v_exp_f32 v67, v67
v_exp_f32 v68, v68
v_mfma_f32_32x32x16_bf16 v[16:31], v[170:173], v[144:147], v[16:31]
v_exp_f32 v69, v69
v_exp_f32 v70, v70
v_exp_f32 v71, v71
v_mfma_f32_32x32x16_bf16 v[32:47], v[170:173], v[148:151], v[32:47]
buffer_load_dwordx4 a[188:191], v2, s[12:15], s65 offen offset:3072 sc1
s_add_u32 s65, s65, 4096
v_exp_f32 v72, v72
v_exp_f32 v73, v73
v_exp_f32 v74, v74
v_smfmac_f32_32x32x32_bf16 v[80:95], v[190:193], v[64:71], v14
v_exp_f32 v75, v75
v_exp_f32 v76, v76
v_exp_f32 v77, v77
v_exp_f32 v78, v78
v_exp_f32 v79, v79
v_exp_f32 v16, v16
v_exp_f32 v17, v17
v_smfmac_f32_32x32x32_bf16 v[80:95], v[194:197], v[72:79], v14
buffer_load_dwordx4 v[190:193], v10, s[12:15], s79 offen sc1
v_exp_f32 v18, v18
v_exp_f32 v19, v19
v_exp_f32 v20, v20
v_mfma_f32_32x32x4_2b_bf16 v[48:79], v[174:175], v[162:163], 0
buffer_load_dwordx4 v[194:197], v11, s[12:15], s79 offen sc1
v_exp_f32 v21, v21
v_exp_f32 v22, v22
v_exp_f32 v23, v23
v_exp_f32 v24, v24
v_exp_f32 v25, v25
v_exp_f32 v26, v26
v_exp_f32 v27, v27
v_exp_f32 v28, v28
v_smfmac_f32_32x32x32_bf16 v[96:111], v[198:201], v[16:23], v14
v_exp_f32 v29, v29
v_exp_f32 v30, v30
v_exp_f32 v31, v31
v_exp_f32 v32, v32
v_exp_f32 v33, v33
v_smfmac_f32_32x32x32_bf16 v[96:111], v[202:205], v[24:31], v14
buffer_load_dwordx4 a[192:195], v2, s[12:15], s65 offen offset:0 sc1
v_exp_f32 v34, v34
v_exp_f32 v35, v35
v_exp_f32 v36, v36
v_mfma_f32_32x32x16_bf16 v[48:63], v[170:173], v[152:155], v[48:63]
v_exp_f32 v37, v37
v_exp_f32 v38, v38
v_exp_f32 v39, v39
v_mfma_f32_32x32x16_bf16 v[64:79], v[170:173], v[156:159], v[64:79]
buffer_load_dwordx4 a[196:199], v2, s[12:15], s65 offen offset:1024 sc1
v_exp_f32 v40, v40
v_exp_f32 v41, v41
v_exp_f32 v42, v42
v_smfmac_f32_32x32x32_bf16 v[112:127], v[198:201], v[32:39], v14
v_exp_f32 v43, v43
v_exp_f32 v44, v44
v_exp_f32 v45, v45
v_exp_f32 v46, v46
v_exp_f32 v47, v47
s_waitcnt vmcnt(14)
v_exp_f32 v48, v48
v_exp_f32 v49, v49
v_smfmac_f32_32x32x32_bf16 v[112:127], v[202:205], v[40:47], v14
buffer_load_dwordx4 v[170:173], v8, s[12:15], s79 offen sc1
v_exp_f32 v50, v50
v_exp_f32 v51, v51
v_exp_f32 v52, v52
v_mfma_f32_32x32x4_2b_bf16 v[16:47], v[180:181], v[160:161], 0
buffer_load_dwordx2 v[174:175], v9, s[12:15], s79 offen sc1
v_exp_f32 v53, v53
v_exp_f32 v54, v54
v_exp_f32 v55, v55
v_exp_f32 v56, v56
v_exp_f32 v57, v57
v_exp_f32 v58, v58
v_exp_f32 v59, v59
v_exp_f32 v60, v60
v_smfmac_f32_32x32x32_bf16 v[128:143], v[198:201], v[48:55], v14
v_exp_f32 v61, v61
v_exp_f32 v62, v62
v_exp_f32 v63, v63
v_exp_f32 v64, v64
v_exp_f32 v65, v65
v_smfmac_f32_32x32x32_bf16 v[128:143], v[202:205], v[56:63], v14
buffer_load_dwordx4 a[200:203], v2, s[12:15], s65 offen offset:2048 sc1
v_exp_f32 v66, v66
v_exp_f32 v67, v67
v_exp_f32 v68, v68
v_mfma_f32_32x32x16_bf16 v[16:31], v[176:179], v[144:147], v[16:31]
v_exp_f32 v69, v69
v_exp_f32 v70, v70
v_exp_f32 v71, v71
v_mfma_f32_32x32x16_bf16 v[32:47], v[176:179], v[148:151], v[32:47]
buffer_load_dwordx4 a[204:207], v2, s[12:15], s65 offen offset:3072 sc1
s_add_u32 s65, s65, 4096
v_exp_f32 v72, v72
v_exp_f32 v73, v73
v_exp_f32 v74, v74
v_smfmac_f32_32x32x32_bf16 v[80:95], v[198:201], v[64:71], v14
v_exp_f32 v75, v75
v_exp_f32 v76, v76
v_exp_f32 v77, v77
v_exp_f32 v78, v78
v_exp_f32 v79, v79
v_exp_f32 v16, v16
v_exp_f32 v17, v17
v_smfmac_f32_32x32x32_bf16 v[80:95], v[202:205], v[72:79], v14
buffer_load_dwordx4 v[198:201], v10, s[12:15], s80 offen sc1
v_exp_f32 v18, v18
v_exp_f32 v19, v19
v_exp_f32 v20, v20
v_mfma_f32_32x32x4_2b_bf16 v[48:79], v[180:181], v[162:163], 0
buffer_load_dwordx4 v[202:205], v11, s[12:15], s80 offen sc1
v_exp_f32 v21, v21
v_exp_f32 v22, v22
v_exp_f32 v23, v23
v_exp_f32 v24, v24
v_exp_f32 v25, v25
v_exp_f32 v26, v26
v_exp_f32 v27, v27
v_exp_f32 v28, v28
v_smfmac_f32_32x32x32_bf16 v[96:111], v[206:209], v[16:23], v14
v_exp_f32 v29, v29
v_exp_f32 v30, v30
v_exp_f32 v31, v31
v_exp_f32 v32, v32
v_exp_f32 v33, v33
v_smfmac_f32_32x32x32_bf16 v[96:111], v[210:213], v[24:31], v14
buffer_load_dwordx4 a[208:211], v2, s[12:15], s65 offen offset:0 sc1
v_exp_f32 v34, v34
v_exp_f32 v35, v35
v_exp_f32 v36, v36
v_mfma_f32_32x32x16_bf16 v[48:63], v[176:179], v[152:155], v[48:63]
v_exp_f32 v37, v37
v_exp_f32 v38, v38
v_exp_f32 v39, v39
v_mfma_f32_32x32x16_bf16 v[64:79], v[176:179], v[156:159], v[64:79]
buffer_load_dwordx4 a[212:215], v2, s[12:15], s65 offen offset:1024 sc1
v_exp_f32 v40, v40
v_exp_f32 v41, v41
v_exp_f32 v42, v42
v_smfmac_f32_32x32x32_bf16 v[112:127], v[206:209], v[32:39], v14
v_exp_f32 v43, v43
v_exp_f32 v44, v44
v_exp_f32 v45, v45
v_exp_f32 v46, v46
v_exp_f32 v47, v47
s_waitcnt vmcnt(14)
v_exp_f32 v48, v48
v_exp_f32 v49, v49
v_smfmac_f32_32x32x32_bf16 v[112:127], v[210:213], v[40:47], v14
buffer_load_dwordx4 v[176:179], v8, s[12:15], s80 offen sc1
v_exp_f32 v50, v50
v_exp_f32 v51, v51
v_exp_f32 v52, v52
v_mfma_f32_32x32x4_2b_bf16 v[16:47], v[168:169], v[160:161], 0
buffer_load_dwordx2 v[180:181], v9, s[12:15], s80 offen sc1
v_exp_f32 v53, v53
v_exp_f32 v54, v54
v_exp_f32 v55, v55
v_exp_f32 v56, v56
v_exp_f32 v57, v57
v_exp_f32 v58, v58
v_exp_f32 v59, v59
v_exp_f32 v60, v60
v_smfmac_f32_32x32x32_bf16 v[128:143], v[206:209], v[48:55], v14
v_exp_f32 v61, v61
v_exp_f32 v62, v62
v_exp_f32 v63, v63
v_exp_f32 v64, v64
v_exp_f32 v65, v65
v_smfmac_f32_32x32x32_bf16 v[128:143], v[210:213], v[56:63], v14
buffer_load_dwordx4 a[216:219], v2, s[12:15], s65 offen offset:2048 sc1
v_exp_f32 v66, v66
v_exp_f32 v67, v67
v_exp_f32 v68, v68
v_mfma_f32_32x32x16_bf16 v[16:31], v[164:167], v[144:147], v[16:31]
v_exp_f32 v69, v69
v_exp_f32 v70, v70
v_exp_f32 v71, v71
v_mfma_f32_32x32x16_bf16 v[32:47], v[164:167], v[148:151], v[32:47]
v_exp_f32 v72, v72
v_exp_f32 v73, v73
v_exp_f32 v74, v74
v_smfmac_f32_32x32x32_bf16 v[80:95], v[206:209], v[64:71], v14
v_exp_f32 v75, v75
v_exp_f32 v76, v76
v_exp_f32 v77, v77
v_exp_f32 v78, v78
v_exp_f32 v79, v79
v_exp_f32 v16, v16
v_exp_f32 v17, v17
v_smfmac_f32_32x32x32_bf16 v[80:95], v[210:213], v[72:79], v14
buffer_load_dwordx4 v[206:209], v10, s[12:15], s81 offen sc1
v_exp_f32 v18, v18
v_exp_f32 v19, v19
v_exp_f32 v20, v20
v_mfma_f32_32x32x4_2b_bf16 v[48:79], v[168:169], v[162:163], 0
buffer_load_dwordx4 v[210:213], v11, s[12:15], s81 offen sc1
v_exp_f32 v21, v21
v_exp_f32 v22, v22
v_exp_f32 v23, v23
v_exp_f32 v24, v24
v_exp_f32 v25, v25
v_exp_f32 v26, v26
v_exp_f32 v27, v27
v_exp_f32 v28, v28
v_smfmac_f32_32x32x32_bf16 v[96:111], v[182:185], v[16:23], v14
v_exp_f32 v29, v29
v_exp_f32 v30, v30
v_exp_f32 v31, v31
v_exp_f32 v32, v32
v_exp_f32 v33, v33
v_smfmac_f32_32x32x32_bf16 v[96:111], v[186:189], v[24:31], v14
buffer_load_dwordx4 a[220:223], v2, s[12:15], s65 offen offset:3072 sc1
s_add_u32 s65, s65, 4096
v_exp_f32 v34, v34
v_exp_f32 v35, v35
v_exp_f32 v36, v36
v_mfma_f32_32x32x16_bf16 v[48:63], v[164:167], v[152:155], v[48:63]
v_exp_f32 v37, v37
v_exp_f32 v38, v38
v_exp_f32 v39, v39
v_mfma_f32_32x32x16_bf16 v[64:79], v[164:167], v[156:159], v[64:79]
buffer_load_dwordx4 a[224:227], v2, s[12:15], s65 offen offset:0 sc1
v_exp_f32 v40, v40
v_exp_f32 v41, v41
v_exp_f32 v42, v42
v_smfmac_f32_32x32x32_bf16 v[112:127], v[182:185], v[32:39], v14
v_exp_f32 v43, v43
v_exp_f32 v44, v44
v_exp_f32 v45, v45
v_exp_f32 v46, v46
v_exp_f32 v47, v47
s_waitcnt vmcnt(13)
v_exp_f32 v48, v48
v_exp_f32 v49, v49
v_smfmac_f32_32x32x32_bf16 v[112:127], v[186:189], v[40:47], v14
buffer_load_dwordx4 v[164:167], v8, s[12:15], s81 offen sc1
v_exp_f32 v50, v50
v_exp_f32 v51, v51
v_exp_f32 v52, v52
v_mfma_f32_32x32x4_2b_bf16 v[16:47], v[174:175], v[160:161], 0
buffer_load_dwordx2 v[168:169], v9, s[12:15], s81 offen sc1
v_exp_f32 v53, v53
v_exp_f32 v54, v54
v_exp_f32 v55, v55
v_exp_f32 v56, v56
v_exp_f32 v57, v57
v_exp_f32 v58, v58
v_exp_f32 v59, v59
v_exp_f32 v60, v60
v_smfmac_f32_32x32x32_bf16 v[128:143], v[182:185], v[48:55], v14
v_exp_f32 v61, v61
v_exp_f32 v62, v62
v_exp_f32 v63, v63
v_exp_f32 v64, v64
v_exp_f32 v65, v65
v_smfmac_f32_32x32x32_bf16 v[128:143], v[186:189], v[56:63], v14
buffer_load_dwordx4 a[228:231], v2, s[12:15], s65 offen offset:1024 sc1
v_exp_f32 v66, v66
v_exp_f32 v67, v67
v_exp_f32 v68, v68
v_mfma_f32_32x32x16_bf16 v[16:31], v[170:173], v[144:147], v[16:31]
v_exp_f32 v69, v69
v_exp_f32 v70, v70
v_exp_f32 v71, v71
v_mfma_f32_32x32x16_bf16 v[32:47], v[170:173], v[148:151], v[32:47]
buffer_load_dwordx4 a[232:235], v2, s[12:15], s65 offen offset:2048 sc1
v_exp_f32 v72, v72
v_exp_f32 v73, v73
v_exp_f32 v74, v74
v_smfmac_f32_32x32x32_bf16 v[80:95], v[182:185], v[64:71], v14
v_exp_f32 v75, v75
v_exp_f32 v76, v76
v_exp_f32 v77, v77
v_exp_f32 v78, v78
v_exp_f32 v79, v79
v_exp_f32 v16, v16
v_exp_f32 v17, v17
v_smfmac_f32_32x32x32_bf16 v[80:95], v[186:189], v[72:79], v14
buffer_load_dwordx4 v[182:185], v10, s[12:15], s82 offen sc1
v_exp_f32 v18, v18
v_exp_f32 v19, v19
v_exp_f32 v20, v20
v_mfma_f32_32x32x4_2b_bf16 v[48:79], v[174:175], v[162:163], 0
buffer_load_dwordx4 v[186:189], v11, s[12:15], s82 offen sc1
v_exp_f32 v21, v21
v_exp_f32 v22, v22
v_exp_f32 v23, v23
v_exp_f32 v24, v24
v_exp_f32 v25, v25
v_exp_f32 v26, v26
v_exp_f32 v27, v27
v_exp_f32 v28, v28
v_smfmac_f32_32x32x32_bf16 v[96:111], v[190:193], v[16:23], v14
v_exp_f32 v29, v29
v_exp_f32 v30, v30
v_exp_f32 v31, v31
v_exp_f32 v32, v32
v_exp_f32 v33, v33
v_smfmac_f32_32x32x32_bf16 v[96:111], v[194:197], v[24:31], v14
buffer_load_dwordx4 a[236:239], v2, s[12:15], s65 offen offset:3072 sc1
s_add_u32 s65, s65, 4096
v_exp_f32 v34, v34
v_exp_f32 v35, v35
v_exp_f32 v36, v36
)ASM"
      R"ASM(
v_mfma_f32_32x32x16_bf16 v[48:63], v[170:173], v[152:155], v[48:63]
v_exp_f32 v37, v37
v_exp_f32 v38, v38
v_exp_f32 v39, v39
v_mfma_f32_32x32x16_bf16 v[64:79], v[170:173], v[156:159], v[64:79]
buffer_load_dwordx4 a[240:243], v2, s[12:15], s65 offen offset:0 sc1
v_exp_f32 v40, v40
v_exp_f32 v41, v41
v_exp_f32 v42, v42
v_smfmac_f32_32x32x32_bf16 v[112:127], v[190:193], v[32:39], v14
v_exp_f32 v43, v43
v_exp_f32 v44, v44
v_exp_f32 v45, v45
v_exp_f32 v46, v46
v_exp_f32 v47, v47
s_waitcnt vmcnt(13)
v_exp_f32 v48, v48
v_exp_f32 v49, v49
v_smfmac_f32_32x32x32_bf16 v[112:127], v[194:197], v[40:47], v14
buffer_load_dwordx4 v[170:173], v8, s[12:15], s82 offen sc1
v_exp_f32 v50, v50
v_exp_f32 v51, v51
v_exp_f32 v52, v52
v_mfma_f32_32x32x4_2b_bf16 v[16:47], v[180:181], v[160:161], 0
buffer_load_dwordx2 v[174:175], v9, s[12:15], s82 offen sc1
v_exp_f32 v53, v53
v_exp_f32 v54, v54
v_exp_f32 v55, v55
v_exp_f32 v56, v56
v_exp_f32 v57, v57
v_exp_f32 v58, v58
v_exp_f32 v59, v59
v_exp_f32 v60, v60
v_smfmac_f32_32x32x32_bf16 v[128:143], v[190:193], v[48:55], v14
v_exp_f32 v61, v61
v_exp_f32 v62, v62
v_exp_f32 v63, v63
v_exp_f32 v64, v64
v_exp_f32 v65, v65
v_smfmac_f32_32x32x32_bf16 v[128:143], v[194:197], v[56:63], v14
buffer_load_dwordx4 a[244:247], v2, s[12:15], s65 offen offset:1024 sc1
v_exp_f32 v66, v66
v_exp_f32 v67, v67
v_exp_f32 v68, v68
v_mfma_f32_32x32x16_bf16 v[16:31], v[176:179], v[144:147], v[16:31]
v_exp_f32 v69, v69
v_exp_f32 v70, v70
v_exp_f32 v71, v71
v_mfma_f32_32x32x16_bf16 v[32:47], v[176:179], v[148:151], v[32:47]
buffer_load_dwordx4 a[248:251], v2, s[12:15], s65 offen offset:2048 sc1
v_exp_f32 v72, v72
v_exp_f32 v73, v73
v_exp_f32 v74, v74
v_smfmac_f32_32x32x32_bf16 v[80:95], v[190:193], v[64:71], v14
v_exp_f32 v75, v75
v_exp_f32 v76, v76
v_exp_f32 v77, v77
v_exp_f32 v78, v78
v_exp_f32 v79, v79
v_exp_f32 v16, v16
v_exp_f32 v17, v17
v_smfmac_f32_32x32x32_bf16 v[80:95], v[194:197], v[72:79], v14
buffer_load_dwordx4 v[190:193], v10, s[12:15], s83 offen sc1
v_exp_f32 v18, v18
v_exp_f32 v19, v19
v_exp_f32 v20, v20
v_mfma_f32_32x32x4_2b_bf16 v[48:79], v[180:181], v[162:163], 0
buffer_load_dwordx4 v[194:197], v11, s[12:15], s83 offen sc1
v_exp_f32 v21, v21
v_exp_f32 v22, v22
v_exp_f32 v23, v23
v_exp_f32 v24, v24
v_exp_f32 v25, v25
v_exp_f32 v26, v26
v_exp_f32 v27, v27
v_exp_f32 v28, v28
v_smfmac_f32_32x32x32_bf16 v[96:111], v[198:201], v[16:23], v14
v_exp_f32 v29, v29
v_exp_f32 v30, v30
v_exp_f32 v31, v31
v_exp_f32 v32, v32
v_exp_f32 v33, v33
v_smfmac_f32_32x32x32_bf16 v[96:111], v[202:205], v[24:31], v14
buffer_load_dwordx4 a[252:255], v2, s[12:15], s65 offen offset:3072 sc1
s_add_u32 s65, s65, 4096
v_exp_f32 v34, v34
v_exp_f32 v35, v35
v_exp_f32 v36, v36
v_mfma_f32_32x32x16_bf16 v[48:63], v[176:179], v[152:155], v[48:63]
v_exp_f32 v37, v37
v_exp_f32 v38, v38
v_exp_f32 v39, v39
v_mfma_f32_32x32x16_bf16 v[64:79], v[176:179], v[156:159], v[64:79]
buffer_load_dwordx4 v[216:219], v2, s[12:15], s65 offen offset:0 sc1
v_exp_f32 v40, v40
v_exp_f32 v41, v41
v_exp_f32 v42, v42
v_smfmac_f32_32x32x32_bf16 v[112:127], v[198:201], v[32:39], v14
v_exp_f32 v43, v43
v_exp_f32 v44, v44
v_exp_f32 v45, v45
v_exp_f32 v46, v46
v_exp_f32 v47, v47
s_waitcnt vmcnt(14)
v_exp_f32 v48, v48
v_exp_f32 v49, v49
v_smfmac_f32_32x32x32_bf16 v[112:127], v[202:205], v[40:47], v14
buffer_load_dwordx4 v[176:179], v8, s[12:15], s83 offen sc1
v_exp_f32 v50, v50
v_exp_f32 v51, v51
v_exp_f32 v52, v52
v_mfma_f32_32x32x4_2b_bf16 v[16:47], v[168:169], v[160:161], 0
buffer_load_dwordx2 v[180:181], v9, s[12:15], s83 offen sc1
v_exp_f32 v53, v53
v_exp_f32 v54, v54
v_exp_f32 v55, v55
v_exp_f32 v56, v56
v_exp_f32 v57, v57
v_exp_f32 v58, v58
v_exp_f32 v59, v59
v_exp_f32 v60, v60
v_smfmac_f32_32x32x32_bf16 v[128:143], v[198:201], v[48:55], v14
v_exp_f32 v61, v61
v_exp_f32 v62, v62
v_exp_f32 v63, v63
v_exp_f32 v64, v64
v_exp_f32 v65, v65
v_smfmac_f32_32x32x32_bf16 v[128:143], v[202:205], v[56:63], v14
buffer_load_dwordx4 v[220:223], v2, s[12:15], s65 offen offset:1024 sc1
v_exp_f32 v66, v66
v_exp_f32 v67, v67
v_exp_f32 v68, v68
v_mfma_f32_32x32x16_bf16 v[16:31], v[164:167], v[144:147], v[16:31]
v_exp_f32 v69, v69
v_exp_f32 v70, v70
v_exp_f32 v71, v71
v_mfma_f32_32x32x16_bf16 v[32:47], v[164:167], v[148:151], v[32:47]
buffer_load_dwordx4 v[224:227], v2, s[12:15], s65 offen offset:2048 sc1
v_exp_f32 v72, v72
v_exp_f32 v73, v73
v_exp_f32 v74, v74
v_smfmac_f32_32x32x32_bf16 v[80:95], v[198:201], v[64:71], v14
v_exp_f32 v75, v75
v_exp_f32 v76, v76
v_exp_f32 v77, v77
v_exp_f32 v78, v78
v_exp_f32 v79, v79
v_exp_f32 v16, v16
v_exp_f32 v17, v17
v_smfmac_f32_32x32x32_bf16 v[80:95], v[202:205], v[72:79], v14
buffer_load_dwordx4 v[198:201], v10, s[12:15], s84 offen sc1
v_exp_f32 v18, v18
v_exp_f32 v19, v19
v_exp_f32 v20, v20
v_mfma_f32_32x32x4_2b_bf16 v[48:79], v[168:169], v[162:163], 0
buffer_load_dwordx4 v[202:205], v11, s[12:15], s84 offen sc1
v_exp_f32 v21, v21
v_exp_f32 v22, v22
v_exp_f32 v23, v23
v_exp_f32 v24, v24
v_exp_f32 v25, v25
v_exp_f32 v26, v26
v_exp_f32 v27, v27
v_exp_f32 v28, v28
v_smfmac_f32_32x32x32_bf16 v[96:111], v[206:209], v[16:23], v14
v_exp_f32 v29, v29
v_exp_f32 v30, v30
v_exp_f32 v31, v31
v_exp_f32 v32, v32
v_exp_f32 v33, v33
v_smfmac_f32_32x32x32_bf16 v[96:111], v[210:213], v[24:31], v14
buffer_load_dwordx4 v[228:231], v2, s[12:15], s65 offen offset:3072 sc1
s_add_u32 s65, s65, 4096
v_exp_f32 v34, v34
v_exp_f32 v35, v35
v_exp_f32 v36, v36
v_mfma_f32_32x32x16_bf16 v[48:63], v[164:167], v[152:155], v[48:63]
v_exp_f32 v37, v37
v_exp_f32 v38, v38
v_exp_f32 v39, v39
v_mfma_f32_32x32x16_bf16 v[64:79], v[164:167], v[156:159], v[64:79]
buffer_load_dwordx4 v[232:235], v2, s[12:15], s65 offen offset:0 sc1
v_exp_f32 v40, v40
v_exp_f32 v41, v41
v_exp_f32 v42, v42
v_smfmac_f32_32x32x32_bf16 v[112:127], v[206:209], v[32:39], v14
v_exp_f32 v43, v43
v_exp_f32 v44, v44
v_exp_f32 v45, v45
v_exp_f32 v46, v46
v_exp_f32 v47, v47
s_waitcnt vmcnt(14)
v_exp_f32 v48, v48
v_exp_f32 v49, v49
v_smfmac_f32_32x32x32_bf16 v[112:127], v[210:213], v[40:47], v14
buffer_load_dwordx4 v[164:167], v8, s[12:15], s84 offen sc1
v_exp_f32 v50, v50
v_exp_f32 v51, v51
v_exp_f32 v52, v52
v_mfma_f32_32x32x4_2b_bf16 v[16:47], v[174:175], v[160:161], 0
buffer_load_dwordx2 v[168:169], v9, s[12:15], s84 offen sc1
v_exp_f32 v53, v53
v_exp_f32 v54, v54
v_exp_f32 v55, v55
v_exp_f32 v56, v56
v_exp_f32 v57, v57
v_exp_f32 v58, v58
v_exp_f32 v59, v59
v_exp_f32 v60, v60
v_smfmac_f32_32x32x32_bf16 v[128:143], v[206:209], v[48:55], v14
v_exp_f32 v61, v61
v_exp_f32 v62, v62
v_exp_f32 v63, v63
v_exp_f32 v64, v64
v_exp_f32 v65, v65
v_smfmac_f32_32x32x32_bf16 v[128:143], v[210:213], v[56:63], v14
buffer_load_dwordx4 v[236:239], v2, s[12:15], s65 offen offset:1024 sc1
v_exp_f32 v66, v66
v_exp_f32 v67, v67
v_exp_f32 v68, v68
v_mfma_f32_32x32x16_bf16 v[16:31], v[170:173], v[144:147], v[16:31]
v_exp_f32 v69, v69
v_exp_f32 v70, v70
v_exp_f32 v71, v71
v_mfma_f32_32x32x16_bf16 v[32:47], v[170:173], v[148:151], v[32:47]
buffer_load_dwordx4 v[240:243], v2, s[12:15], s65 offen offset:2048 sc1
v_exp_f32 v72, v72
v_exp_f32 v73, v73
v_exp_f32 v74, v74
v_smfmac_f32_32x32x32_bf16 v[80:95], v[206:209], v[64:71], v14
v_exp_f32 v75, v75
v_exp_f32 v76, v76
v_exp_f32 v77, v77
v_exp_f32 v78, v78
v_exp_f32 v79, v79
v_exp_f32 v16, v16
v_exp_f32 v17, v17
v_smfmac_f32_32x32x32_bf16 v[80:95], v[210:213], v[72:79], v14
buffer_load_dwordx4 v[206:209], v10, s[12:15], s85 offen sc1
v_exp_f32 v18, v18
v_exp_f32 v19, v19
v_exp_f32 v20, v20
v_mfma_f32_32x32x4_2b_bf16 v[48:79], v[174:175], v[162:163], 0
buffer_load_dwordx4 v[210:213], v11, s[12:15], s85 offen sc1
v_exp_f32 v21, v21
v_exp_f32 v22, v22
v_exp_f32 v23, v23
v_exp_f32 v24, v24
v_exp_f32 v25, v25
v_exp_f32 v26, v26
v_exp_f32 v27, v27
v_exp_f32 v28, v28
v_smfmac_f32_32x32x32_bf16 v[96:111], v[182:185], v[16:23], v14
v_exp_f32 v29, v29
v_exp_f32 v30, v30
v_exp_f32 v31, v31
v_exp_f32 v32, v32
v_exp_f32 v33, v33
v_smfmac_f32_32x32x32_bf16 v[96:111], v[186:189], v[24:31], v14
buffer_load_dwordx4 v[244:247], v2, s[12:15], s65 offen offset:3072 sc1
s_add_u32 s65, s65, 4096
v_exp_f32 v34, v34
v_exp_f32 v35, v35
v_exp_f32 v36, v36
v_mfma_f32_32x32x16_bf16 v[48:63], v[170:173], v[152:155], v[48:63]
v_exp_f32 v37, v37
v_exp_f32 v38, v38
v_exp_f32 v39, v39
v_mfma_f32_32x32x16_bf16 v[64:79], v[170:173], v[156:159], v[64:79]
buffer_load_dwordx4 v[248:251], v2, s[12:15], s65 offen offset:0 sc1
v_exp_f32 v40, v40
v_exp_f32 v41, v41
v_exp_f32 v42, v42
v_smfmac_f32_32x32x32_bf16 v[112:127], v[182:185], v[32:39], v14
v_exp_f32 v43, v43
v_exp_f32 v44, v44
v_exp_f32 v45, v45
v_exp_f32 v46, v46
v_exp_f32 v47, v47
s_waitcnt vmcnt(14)
v_exp_f32 v48, v48
v_exp_f32 v49, v49
v_smfmac_f32_32x32x32_bf16 v[112:127], v[186:189], v[40:47], v14
buffer_load_dwordx4 v[170:173], v8, s[12:15], s85 offen sc1
v_exp_f32 v50, v50
v_exp_f32 v51, v51
v_exp_f32 v52, v52
v_mfma_f32_32x32x4_2b_bf16 v[16:47], v[180:181], v[160:161], 0
buffer_load_dwordx2 v[174:175], v9, s[12:15], s85 offen sc1
v_exp_f32 v53, v53
v_exp_f32 v54, v54
v_exp_f32 v55, v55
v_exp_f32 v56, v56
v_exp_f32 v57, v57
v_exp_f32 v58, v58
v_exp_f32 v59, v59
v_exp_f32 v60, v60
v_smfmac_f32_32x32x32_bf16 v[128:143], v[182:185], v[48:55], v14
v_exp_f32 v61, v61
v_exp_f32 v62, v62
v_exp_f32 v63, v63
v_exp_f32 v64, v64
v_exp_f32 v65, v65
v_smfmac_f32_32x32x32_bf16 v[128:143], v[186:189], v[56:63], v14
buffer_load_dwordx4 v[252:255], v2, s[12:15], s65 offen offset:1024 sc1
v_exp_f32 v66, v66
v_exp_f32 v67, v67
v_exp_f32 v68, v68
v_mfma_f32_32x32x16_bf16 v[16:31], v[176:179], v[144:147], v[16:31]
v_exp_f32 v69, v69
v_exp_f32 v70, v70
v_exp_f32 v71, v71
v_mfma_f32_32x32x16_bf16 v[32:47], v[176:179], v[148:151], v[32:47]
v_exp_f32 v72, v72
v_exp_f32 v73, v73
v_exp_f32 v74, v74
v_smfmac_f32_32x32x32_bf16 v[80:95], v[182:185], v[64:71], v14
v_exp_f32 v75, v75
v_exp_f32 v76, v76
v_exp_f32 v77, v77
v_exp_f32 v78, v78
v_exp_f32 v79, v79
v_exp_f32 v16, v16
v_exp_f32 v17, v17
v_smfmac_f32_32x32x32_bf16 v[80:95], v[186:189], v[72:79], v14
v_exp_f32 v18, v18
v_exp_f32 v19, v19
v_exp_f32 v20, v20
v_mfma_f32_32x32x4_2b_bf16 v[48:79], v[180:181], v[162:163], 0
v_exp_f32 v21, v21
v_exp_f32 v22, v22
v_exp_f32 v23, v23
v_exp_f32 v24, v24
v_exp_f32 v25, v25
v_exp_f32 v26, v26
v_exp_f32 v27, v27
v_exp_f32 v28, v28
v_smfmac_f32_32x32x32_bf16 v[96:111], v[190:193], v[16:23], v14
v_exp_f32 v29, v29
v_exp_f32 v30, v30
v_exp_f32 v31, v31
v_exp_f32 v32, v32
v_exp_f32 v33, v33
v_smfmac_f32_32x32x32_bf16 v[96:111], v[194:197], v[24:31], v14
v_exp_f32 v34, v34
v_exp_f32 v35, v35
v_exp_f32 v36, v36
v_mfma_f32_32x32x16_bf16 v[48:63], v[176:179], v[152:155], v[48:63]
v_exp_f32 v37, v37
v_exp_f32 v38, v38
v_exp_f32 v39, v39
v_mfma_f32_32x32x16_bf16 v[64:79], v[176:179], v[156:159], v[64:79]
v_exp_f32 v40, v40
v_exp_f32 v41, v41
v_exp_f32 v42, v42
v_smfmac_f32_32x32x32_bf16 v[112:127], v[190:193], v[32:39], v14
v_exp_f32 v43, v43
v_exp_f32 v44, v44
v_exp_f32 v45, v45
v_exp_f32 v46, v46
v_exp_f32 v47, v47
s_waitcnt vmcnt(9)
v_exp_f32 v48, v48
v_exp_f32 v49, v49
v_smfmac_f32_32x32x32_bf16 v[112:127], v[194:197], v[40:47], v14
v_exp_f32 v50, v50
v_exp_f32 v51, v51
v_exp_f32 v52, v52
v_mfma_f32_32x32x4_2b_bf16 v[16:47], v[168:169], v[160:161], 0
v_exp_f32 v53, v53
v_exp_f32 v54, v54
v_exp_f32 v55, v55
v_exp_f32 v56, v56
v_exp_f32 v57, v57
v_exp_f32 v58, v58
v_exp_f32 v59, v59
v_exp_f32 v60, v60
v_smfmac_f32_32x32x32_bf16 v[128:143], v[190:193], v[48:55], v14
v_exp_f32 v61, v61
v_exp_f32 v62, v62
v_exp_f32 v63, v63
v_exp_f32 v64, v64
v_exp_f32 v65, v65
v_smfmac_f32_32x32x32_bf16 v[128:143], v[194:197], v[56:63], v14
v_exp_f32 v66, v66
v_exp_f32 v67, v67
v_exp_f32 v68, v68
v_mfma_f32_32x32x16_bf16 v[16:31], v[164:167], v[144:147], v[16:31]
v_exp_f32 v69, v69
v_exp_f32 v70, v70
v_exp_f32 v71, v71
v_mfma_f32_32x32x16_bf16 v[32:47], v[164:167], v[148:151], v[32:47]
v_exp_f32 v72, v72
v_exp_f32 v73, v73
v_exp_f32 v74, v74
v_smfmac_f32_32x32x32_bf16 v[80:95], v[190:193], v[64:71], v14
v_exp_f32 v75, v75
v_exp_f32 v76, v76
v_exp_f32 v77, v77
v_exp_f32 v78, v78
)ASM"
      R"ASM(
v_exp_f32 v79, v79
v_exp_f32 v16, v16
v_exp_f32 v17, v17
v_smfmac_f32_32x32x32_bf16 v[80:95], v[194:197], v[72:79], v14
v_exp_f32 v18, v18
v_exp_f32 v19, v19
v_exp_f32 v20, v20
v_mfma_f32_32x32x4_2b_bf16 v[48:79], v[168:169], v[162:163], 0
v_exp_f32 v21, v21
v_exp_f32 v22, v22
v_exp_f32 v23, v23
v_exp_f32 v24, v24
v_exp_f32 v25, v25
v_exp_f32 v26, v26
v_exp_f32 v27, v27
v_exp_f32 v28, v28
v_smfmac_f32_32x32x32_bf16 v[96:111], v[198:201], v[16:23], v14
v_exp_f32 v29, v29
v_exp_f32 v30, v30
v_exp_f32 v31, v31
v_exp_f32 v32, v32
v_exp_f32 v33, v33
v_smfmac_f32_32x32x32_bf16 v[96:111], v[202:205], v[24:31], v14
v_exp_f32 v34, v34
v_exp_f32 v35, v35
v_exp_f32 v36, v36
v_mfma_f32_32x32x16_bf16 v[48:63], v[164:167], v[152:155], v[48:63]
v_exp_f32 v37, v37
v_exp_f32 v38, v38
v_exp_f32 v39, v39
v_mfma_f32_32x32x16_bf16 v[64:79], v[164:167], v[156:159], v[64:79]
v_exp_f32 v40, v40
v_exp_f32 v41, v41
v_exp_f32 v42, v42
v_smfmac_f32_32x32x32_bf16 v[112:127], v[198:201], v[32:39], v14
v_exp_f32 v43, v43
v_exp_f32 v44, v44
v_exp_f32 v45, v45
v_exp_f32 v46, v46
v_exp_f32 v47, v47
s_waitcnt vmcnt(1)
v_exp_f32 v48, v48
v_exp_f32 v49, v49
v_smfmac_f32_32x32x32_bf16 v[112:127], v[202:205], v[40:47], v14
v_exp_f32 v50, v50
v_exp_f32 v51, v51
v_exp_f32 v52, v52
v_mfma_f32_32x32x4_2b_bf16 v[16:47], v[174:175], v[160:161], 0
v_exp_f32 v53, v53
v_exp_f32 v54, v54
v_exp_f32 v55, v55
v_exp_f32 v56, v56
v_exp_f32 v57, v57
v_exp_f32 v58, v58
v_exp_f32 v59, v59
v_exp_f32 v60, v60
v_smfmac_f32_32x32x32_bf16 v[128:143], v[198:201], v[48:55], v14
v_exp_f32 v61, v61
v_exp_f32 v62, v62
v_exp_f32 v63, v63
v_exp_f32 v64, v64
v_exp_f32 v65, v65
v_smfmac_f32_32x32x32_bf16 v[128:143], v[202:205], v[56:63], v14
v_exp_f32 v66, v66
v_exp_f32 v67, v67
v_exp_f32 v68, v68
v_mfma_f32_32x32x16_bf16 v[16:31], v[170:173], v[144:147], v[16:31]
v_exp_f32 v69, v69
v_exp_f32 v70, v70
v_exp_f32 v71, v71
v_mfma_f32_32x32x16_bf16 v[32:47], v[170:173], v[148:151], v[32:47]
v_exp_f32 v72, v72
v_exp_f32 v73, v73
v_exp_f32 v74, v74
v_smfmac_f32_32x32x32_bf16 v[80:95], v[198:201], v[64:71], v14
v_exp_f32 v75, v75
v_exp_f32 v76, v76
v_exp_f32 v77, v77
v_exp_f32 v78, v78
v_exp_f32 v79, v79
v_exp_f32 v16, v16
v_exp_f32 v17, v17
v_smfmac_f32_32x32x32_bf16 v[80:95], v[202:205], v[72:79], v14
v_exp_f32 v18, v18
v_exp_f32 v19, v19
v_exp_f32 v20, v20
v_mfma_f32_32x32x4_2b_bf16 v[48:79], v[174:175], v[162:163], 0
v_exp_f32 v21, v21
v_exp_f32 v22, v22
v_exp_f32 v23, v23
v_exp_f32 v24, v24
v_exp_f32 v25, v25
v_exp_f32 v26, v26
v_exp_f32 v27, v27
v_exp_f32 v28, v28
v_smfmac_f32_32x32x32_bf16 v[96:111], v[206:209], v[16:23], v14
v_exp_f32 v29, v29
v_exp_f32 v30, v30
v_exp_f32 v31, v31
v_exp_f32 v32, v32
v_exp_f32 v33, v33
v_smfmac_f32_32x32x32_bf16 v[96:111], v[210:213], v[24:31], v14
v_exp_f32 v34, v34
v_exp_f32 v35, v35
v_exp_f32 v36, v36
v_mfma_f32_32x32x16_bf16 v[48:63], v[170:173], v[152:155], v[48:63]
v_exp_f32 v37, v37
v_exp_f32 v38, v38
v_exp_f32 v39, v39
v_mfma_f32_32x32x16_bf16 v[64:79], v[170:173], v[156:159], v[64:79]
v_exp_f32 v40, v40
v_exp_f32 v41, v41
v_exp_f32 v42, v42
v_smfmac_f32_32x32x32_bf16 v[112:127], v[206:209], v[32:39], v14
v_exp_f32 v43, v43
v_exp_f32 v44, v44
v_exp_f32 v45, v45
v_exp_f32 v46, v46
v_exp_f32 v47, v47
v_exp_f32 v48, v48
v_exp_f32 v49, v49
v_smfmac_f32_32x32x32_bf16 v[112:127], v[210:213], v[40:47], v14
s_add_u32 s50, s47, 1
s_and_b32 s50, s50, 3
s_mul_i32 s50, s50, 9216
s_add_u32 s50, s50, 102400
v_add_u32 v15, s50, v2
ds_write_b128 v15, v[96:99] offset:0
ds_write_b128 v15, v[100:103] offset:1024
ds_write_b128 v15, v[104:107] offset:2048
v_exp_f32 v50, v50
v_exp_f32 v51, v51
v_exp_f32 v52, v52
v_exp_f32 v53, v53
v_exp_f32 v54, v54
v_exp_f32 v55, v55
v_exp_f32 v56, v56
v_exp_f32 v57, v57
v_exp_f32 v58, v58
v_exp_f32 v59, v59
v_exp_f32 v60, v60
v_smfmac_f32_32x32x32_bf16 v[128:143], v[206:209], v[48:55], v14
s_add_u32 s50, s47, 2
s_and_b32 s50, s50, 3
s_mul_i32 s50, s50, 9216
s_add_u32 s50, s50, 102400
v_add_u32 v15, s50, v2
v_exp_f32 v61, v61
v_exp_f32 v62, v62
v_exp_f32 v63, v63
v_exp_f32 v64, v64
v_exp_f32 v65, v65
v_smfmac_f32_32x32x32_bf16 v[128:143], v[210:213], v[56:63], v14
ds_write_b128 v15, v[112:115] offset:3072
ds_write_b128 v15, v[116:119] offset:4096
ds_write_b128 v15, v[120:123] offset:5120
v_exp_f32 v66, v66
v_exp_f32 v67, v67
v_exp_f32 v68, v68
v_exp_f32 v69, v69
v_exp_f32 v70, v70
v_exp_f32 v71, v71
v_exp_f32 v72, v72
v_exp_f32 v73, v73
v_exp_f32 v74, v74
v_smfmac_f32_32x32x32_bf16 v[80:95], v[206:209], v[64:71], v14
v_exp_f32 v75, v75
v_exp_f32 v76, v76
v_exp_f32 v77, v77
v_exp_f32 v78, v78
v_exp_f32 v79, v79
s_add_u32 s50, s47, 3
s_and_b32 s50, s50, 3
s_mul_i32 s50, s50, 9216
s_add_u32 s50, s50, 102400
v_add_u32 v15, s50, v2
v_smfmac_f32_32x32x32_bf16 v[80:95], v[210:213], v[72:79], v14
ds_write_b128 v15, v[128:131] offset:6144
ds_write_b128 v15, v[132:135] offset:7168
ds_write_b128 v15, v[136:139] offset:8192
s_mul_i32 s50, s47, 9216
s_add_u32 s50, s50, 102400
v_add_u32 v15, s50, v2
s_waitcnt lgkmcnt(0)
s_barrier
ds_read_b128 v[16:19], v15 offset:0
ds_read_b128 v[20:23], v15 offset:1024
ds_read_b128 v[24:27], v15 offset:2048
ds_read_b128 v[28:31], v15 offset:3072
ds_read_b128 v[32:35], v15 offset:4096
ds_read_b128 v[36:39], v15 offset:5120
ds_read_b128 v[40:43], v15 offset:6144
ds_read_b128 v[44:47], v15 offset:7168
ds_read_b128 v[48:51], v15 offset:8192
s_waitcnt lgkmcnt(6)
v_pk_add_f32 v[80:81], v[80:81], v[16:17]
v_pk_add_f32 v[82:83], v[82:83], v[18:19]
v_pk_add_f32 v[84:85], v[84:85], v[20:21]
v_pk_add_f32 v[86:87], v[86:87], v[22:23]
v_pk_add_f32 v[88:89], v[88:89], v[24:25]
v_pk_add_f32 v[90:91], v[90:91], v[26:27]
s_waitcnt lgkmcnt(3)
v_pk_add_f32 v[80:81], v[80:81], v[28:29]
v_pk_add_f32 v[82:83], v[82:83], v[30:31]
v_pk_add_f32 v[84:85], v[84:85], v[32:33]
v_pk_add_f32 v[86:87], v[86:87], v[34:35]
v_pk_add_f32 v[88:89], v[88:89], v[36:37]
v_pk_add_f32 v[90:91], v[90:91], v[38:39]
s_waitcnt lgkmcnt(0)
v_pk_add_f32 v[80:81], v[80:81], v[40:41]
v_pk_add_f32 v[82:83], v[82:83], v[42:43]
v_pk_add_f32 v[84:85], v[84:85], v[44:45]
v_pk_add_f32 v[86:87], v[86:87], v[46:47]
v_pk_add_f32 v[88:89], v[88:89], v[48:49]
v_pk_add_f32 v[90:91], v[90:91], v[50:51]
v_mov_b32 v53, v88
v_mov_b32 v52, v88
s_nop 1
v_permlane32_swap_b32 v53, v52
v_pk_mul_f32 v[54:55], v[80:81], 0
v_pk_fma_f32 v[54:55], v[82:83], 0, v[54:55]
v_pk_fma_f32 v[54:55], v[84:85], 0, v[54:55]
v_pk_fma_f32 v[54:55], v[86:87], 0, v[54:55]
v_pk_fma_f32 v[54:55], v[88:89], 0, v[54:55]
v_pk_fma_f32 v[54:55], v[90:91], 0, v[54:55]
v_cmp_nle_f32 vcc, 0x21800000, v52
v_add_f32 v54, v54, v55
v_mov_b32 v53, 0x71800000
v_cmp_nle_f32 s[50:51], v52, v53
v_cmp_u_f32 s[52:53], v54, v54
s_or_b64 vcc, vcc, s[50:51]
s_or_b64 vcc, vcc, s[52:53]
s_cbranch_vccz .Lmk_ok_34
s_mov_b32 s50, s63
v_lshrrev_b32 v60, 5, v0
v_lshl_add_u32 v60, v60, 30, v9
buffer_load_dwordx4 v[32:35], v8, s[12:15], s50 offen sc1
buffer_load_dwordx2 v[36:37], v60, s[12:15], s50 offen sc1
v_mov_b32 v80, 0
v_mov_b32 v81, 0
v_mov_b32 v82, 0
v_mov_b32 v83, 0
v_mov_b32 v84, 0
v_mov_b32 v85, 0
v_mov_b32 v86, 0
v_mov_b32 v87, 0
v_mov_b32 v88, 0
v_mov_b32 v89, 0
v_mov_b32 v90, 0
v_mov_b32 v91, 0
v_mov_b32 v92, 0
v_mov_b32 v93, 0
v_mov_b32 v94, 0
v_mov_b32 v95, 0
v_mov_b32 v57, 0xff800000
s_mov_b32 s50, 4718592
s_mov_b32 s51, 64
.Lmk_fb_35:
buffer_load_dwordx4 v[38:41], v8, s[12:15], s50 offen sc1
buffer_load_dwordx2 v[42:43], v9, s[12:15], s50 offen sc1
buffer_load_dwordx4 v[48:51], v10, s[12:15], s50 offen sc1
buffer_load_dwordx4 v[52:55], v11, s[12:15], s50 offen sc1
s_waitcnt vmcnt(0)
v_mfma_f32_32x32x16_bf16 v[16:31], v[38:41], v[32:35], 0
v_mfma_f32_32x32x8_bf16 v[16:31], v[42:43], v[36:37], v[16:31]
s_nop 11
v_max3_f32 v56, v16, v17, v57
v_max3_f32 v56, v18, v19, v56
v_max3_f32 v56, v20, v21, v56
v_max3_f32 v56, v22, v23, v56
v_max3_f32 v56, v24, v25, v56
v_max3_f32 v56, v26, v27, v56
v_max3_f32 v56, v28, v29, v56
v_max3_f32 v56, v30, v31, v56
v_mov_b32 v58, v56
s_nop 1
v_permlane32_swap_b32 v56, v58
v_max_f32 v56, v56, v58
v_sub_f32 v59, v57, v56
v_exp_f32 v59, v59
v_mov_b32 v57, v56
v_sub_f32 v16, v16, v56
v_sub_f32 v17, v17, v56
v_sub_f32 v18, v18, v56
v_sub_f32 v19, v19, v56
v_sub_f32 v20, v20, v56
v_sub_f32 v21, v21, v56
v_sub_f32 v22, v22, v56
v_sub_f32 v23, v23, v56
v_sub_f32 v24, v24, v56
v_sub_f32 v25, v25, v56
v_sub_f32 v26, v26, v56
v_sub_f32 v27, v27, v56
v_sub_f32 v28, v28, v56
v_sub_f32 v29, v29, v56
v_sub_f32 v30, v30, v56
v_sub_f32 v31, v31, v56
v_exp_f32 v16, v16
v_exp_f32 v17, v17
v_exp_f32 v18, v18
v_exp_f32 v19, v19
v_exp_f32 v20, v20
v_exp_f32 v21, v21
v_exp_f32 v22, v22
v_exp_f32 v23, v23
v_exp_f32 v24, v24
v_exp_f32 v25, v25
v_exp_f32 v26, v26
v_exp_f32 v27, v27
v_exp_f32 v28, v28
v_exp_f32 v29, v29
v_exp_f32 v30, v30
v_exp_f32 v31, v31
v_mul_f32 v80, v80, v59
v_mul_f32 v81, v81, v59
v_mul_f32 v82, v82, v59
v_mul_f32 v83, v83, v59
v_mul_f32 v84, v84, v59
v_mul_f32 v85, v85, v59
v_mul_f32 v86, v86, v59
v_mul_f32 v87, v87, v59
v_mul_f32 v88, v88, v59
v_mul_f32 v89, v89, v59
v_mul_f32 v90, v90, v59
v_mul_f32 v91, v91, v59
s_nop 1
v_smfmac_f32_32x32x32_bf16 v[80:95], v[48:51], v[16:23], v14
v_smfmac_f32_32x32x32_bf16 v[80:95], v[52:55], v[24:31], v14
s_add_u32 s50, s50, 82048
s_sub_u32 s51, s51, 1
s_cmp_lg_u32 s51, 0
s_nop 11
s_cbranch_scc1 .Lmk_fb_35
v_mov_b32 v53, v88
v_mov_b32 v52, v88
s_nop 1
v_permlane32_swap_b32 v53, v52
.Lmk_ok_34:
v_rcp_f32 v52, v52
s_nop 0
v_mul_f32 v80, v80, v52
v_mul_f32 v81, v81, v52
v_mul_f32 v82, v82, v52
v_mul_f32 v83, v83, v52
v_mul_f32 v84, v84, v52
v_mul_f32 v85, v85, v52
v_mul_f32 v86, v86, v52
v_mul_f32 v87, v87, v52
v_mul_f32 v88, v88, v52
v_mul_f32 v89, v89, v52
v_mul_f32 v90, v90, v52
v_mul_f32 v91, v91, v52
v_cvt_pk_bf16_f32 v56, v80, v81
v_cvt_pk_bf16_f32 v57, v82, v83
v_cvt_pk_bf16_f32 v58, v84, v85
v_cvt_pk_bf16_f32 v59, v86, v87
v_cvt_pk_bf16_f32 v60, v88, v89
v_cvt_pk_bf16_f32 v61, v90, v91
s_mov_b32 s50, 131072
buffer_store_dwordx4 v[56:59], v214, s[12:15], s50 offen sc1
s_mov_b32 s50, 32768
buffer_store_dwordx2 v[60:61], v215, s[12:15], s50 offen sc1
s_waitcnt vmcnt(0)
s_add_u32 s68, s68, 131072
v_mov_b32 v4, s44
v_mov_b32 v5, s45
s_mov_b64 exec, 0xffff
buffer_store_dwordx2 v[4:5], v3, s[12:15], s68 offen sc1
s_mov_b64 exec, -1
s_mul_i32 s50, s47, 25600
v_add_u32 v32, s50, v2
ds_read_b128 v[112:115], v32 offset:0
ds_read_b128 v[116:119], v32 offset:1024
ds_read_b128 v[120:123], v32 offset:2048
ds_read_b128 v[124:127], v32 offset:3072
ds_read_b128 v[128:131], v32 offset:4096
ds_read_b128 v[132:135], v32 offset:5120
ds_read_b128 v[136:139], v32 offset:6144
ds_read_b128 v[140:143], v32 offset:7168
ds_read_b128 v[144:147], v32 offset:8192
ds_read_b128 v[148:151], v32 offset:9216
ds_read_b128 v[152:155], v32 offset:10240
ds_read_b128 v[156:159], v32 offset:11264
ds_read_b128 v[160:163], v32 offset:12288
ds_read_b128 v[164:167], v32 offset:13312
ds_read_b128 v[168:171], v32 offset:14336
ds_read_b128 v[172:175], v32 offset:15360
ds_read_b128 v[176:179], v32 offset:16384
ds_read_b128 v[180:183], v32 offset:17408
ds_read_b128 v[184:187], v32 offset:18432
ds_read_b128 v[188:191], v32 offset:19456
ds_read_b128 v[192:195], v32 offset:20480
ds_read_b128 v[196:199], v32 offset:21504
ds_read_b128 v[200:203], v32 offset:22528
ds_read_b128 v[204:207], v32 offset:23552
ds_read_b128 v[208:211], v32 offset:24576
s_lshl_b32 s72, s48, 1
s_add_u32 s72, s72, s91
s_lshl_b32 s70, s48, 3
s_lshl_b32 s50, s91, 1
)ASM"
      R"ASM(
s_add_u32 s70, s70, s50
s_lshr_b32 s71, s47, 1
v_and_b32 v33, 15, v0
v_lshrrev_b32 v34, 4, v0
v_lshrrev_b32 v35, 5, v0
s_and_b32 s51, s47, 1
s_lshl_b32 s51, s51, 4
v_add_u32 v36, s51, v33
s_add_u32 s52, s70, s90
s_mul_i32 s53, s52, 40960
s_mul_i32 s54, s89, 160
s_add_u32 s53, s53, s54
v_mul_u32_u24 v37, 1280, v36
v_add_u32 v37, s53, v37
s_lshl_b32 s54, s71, 6
v_lshl_add_u32 v20, v34, 4, v37
v_add_u32 v20, s54, v20
v_and_b32 v38, 1, v34
v_lshlrev_b32 v38, 3, v38
v_lshl_add_u32 v38, v35, 30, v38
s_lshl_b32 s54, s71, 4
s_add_u32 s54, s54, 128
v_add3_u32 v21, v37, v38, s54
v_lshlrev_b32 v22, 8, v34
v_lshl_add_u32 v22, v33, 3, v22
s_mul_i32 s53, s52, 40960
s_mul_i32 s54, s89, 5120
s_add_u32 s53, s53, s54
s_add_u32 s53, s53, 13631488
v_lshl_add_u32 v37, v36, 4, s53
s_lshl_b32 s54, s71, 11
v_lshl_add_u32 v23, v34, 9, v37
v_add_u32 v23, s54, v23
s_lshl_b32 s54, s71, 9
s_add_u32 s54, s54, 4096
v_add3_u32 v24, v37, v38, s54
s_lshl_b32 s53, s52, 12
s_lshl_b32 s54, s51, 7
s_add_u32 s53, s53, s54
s_lshl_b32 s54, s89, 8
s_add_u32 s53, s53, s54
s_lshl_b32 s54, s71, 7
s_add_u32 s53, s53, s54
s_add_u32 s53, s53, 16252928
v_lshl_add_u32 v25, v33, 3, s53
v_lshl_add_u32 v25, v35, 2, v25
s_waitcnt lgkmcnt(0)
s_barrier
buffer_load_dwordx4 v[26:29], v20, s[4:7], 0 offen
buffer_load_dwordx2 v[30:31], v21, s[4:7], 0 offen
s_xor_b32 s50, s47, 1
s_mul_i32 s50, s50, 6144
s_add_u32 s50, s50, 81920
v_add_u32 v212, s50, v2
s_xor_b32 s50, s47, 2
s_mul_i32 s50, s50, 6144
s_add_u32 s50, s50, 83968
v_add_u32 v213, s50, v2
s_xor_b32 s50, s47, 3
s_mul_i32 s50, s50, 6144
s_add_u32 s50, s50, 86016
v_add_u32 v214, s50, v2
s_lshl_b32 s50, s47, 11
s_and_b32 s51, s47, 1
s_lshl_b32 s51, s51, 10
s_add_u32 s50, s50, s51
s_add_u32 s50, s50, 106496
v_add_u32 v215, s50, v2
s_lshl_b32 s50, s48, 2
s_lshl_b32 s51, s91, 1
s_add_u32 s50, s50, s51
s_add_u32 s50, s50, s90
s_lshl_b32 s50, s50, 2
s_add_u32 s50, s50, s47
s_lshl_b32 s50, s50, 10
s_add_u32 s50, s50, 262144
v_and_b32 v16, 7, v0
v_lshlrev_b32 v16, 7, v16
s_add_u32 s51, s70, s90
s_lshl_b32 s51, s51, 1
s_and_b32 s52, s47, 1
s_xor_b32 s52, s52, 0
s_add_u32 s51, s51, s52
s_add_u32 s51, s51, 0
s_lshl_b32 s52, s51, 14
s_lshl_b32 s53, s47, 12
s_add_u32 s52, s52, s53
s_add_u32 s73, s52, 10485760
s_lshl_b32 s52, s51, 12
s_lshl_b32 s53, s47, 10
s_add_u32 s52, s52, s53
s_add_u32 s75, s52, 12582912
s_add_u32 s51, s70, s90
s_lshl_b32 s51, s51, 1
s_and_b32 s52, s47, 1
s_xor_b32 s52, s52, 1
s_add_u32 s51, s51, s52
s_add_u32 s51, s51, 0
s_lshl_b32 s52, s51, 14
s_lshl_b32 s53, s47, 12
s_add_u32 s52, s52, s53
s_add_u32 s74, s52, 10485760
s_lshl_b32 s52, s51, 12
s_lshl_b32 s53, s47, 10
s_add_u32 s52, s52, s53
s_add_u32 s76, s52, 12582912
s_lshl_b32 s51, s72, 4
s_and_b32 s52, s49, 15
s_add_u32 s51, s51, s52
s_lshl_b32 s51, s51, 2
s_add_u32 s51, s51, s47
s_lshl_b32 s51, s51, 7
s_add_u32 s77, s51, 524288
.Lmk_poll_36:
buffer_load_dwordx2 v[18:19], v16, s[12:15], s50 offen sc1
s_waitcnt vmcnt(0)
v_cmp_ne_u64 vcc, s[44:45], v[18:19]
s_nop 0
s_cbranch_vccz .Lmk_polld_37
s_sub_u32 s46, s46, 1
s_cbranch_scc0 .Lmk_poll_36
s_mov_b32 s46, 0
.Lmk_polld_37:
buffer_load_dwordx4 v[72:75], v2, s[12:15], s73 offen offset:0 sc1
buffer_load_dwordx4 v[76:79], v2, s[12:15], s74 offen offset:0 sc1
buffer_load_dwordx4 v[80:83], v2, s[12:15], s73 offen offset:1024 sc1
buffer_load_dwordx4 v[84:87], v2, s[12:15], s74 offen offset:1024 sc1
buffer_load_dwordx4 v[88:91], v2, s[12:15], s73 offen offset:2048 sc1
buffer_load_dwordx4 v[92:95], v2, s[12:15], s74 offen offset:2048 sc1
buffer_load_dwordx4 v[96:99], v2, s[12:15], s73 offen offset:3072 sc1
buffer_load_dwordx4 v[100:103], v2, s[12:15], s74 offen offset:3072 sc1
buffer_load_dwordx2 v[104:105], v22, s[12:15], s75 offen offset:0 sc1
buffer_load_dwordx2 v[106:107], v22, s[12:15], s75 offen offset:128 sc1
buffer_load_dwordx2 v[108:109], v22, s[12:15], s76 offen offset:0 sc1
buffer_load_dwordx2 v[110:111], v22, s[12:15], s76 offen offset:128 sc1
s_waitcnt vmcnt(10)
v_mfma_f32_16x16x32_bf16 v[52:55], v[112:115], v[76:79], 0
v_mfma_f32_16x16x32_bf16 v[32:35], v[112:115], v[72:75], 0
v_mfma_f32_16x16x32_bf16 v[56:59], v[116:119], v[76:79], 0
v_mfma_f32_16x16x32_bf16 v[36:39], v[116:119], v[72:75], 0
v_mfma_f32_16x16x32_bf16 v[60:63], v[120:123], v[76:79], 0
v_mfma_f32_16x16x32_bf16 v[40:43], v[120:123], v[72:75], 0
v_mfma_f32_16x16x32_bf16 v[64:67], v[124:127], v[76:79], 0
v_mfma_f32_16x16x32_bf16 v[44:47], v[124:127], v[72:75], 0
v_mfma_f32_16x16x32_bf16 v[68:71], v[128:131], v[76:79], 0
v_mfma_f32_16x16x32_bf16 v[48:51], v[128:131], v[72:75], 0
s_waitcnt vmcnt(8)
v_mfma_f32_16x16x32_bf16 v[52:55], v[132:135], v[84:87], v[52:55]
v_mfma_f32_16x16x32_bf16 v[32:35], v[132:135], v[80:83], v[32:35]
v_mfma_f32_16x16x32_bf16 v[56:59], v[136:139], v[84:87], v[56:59]
v_mfma_f32_16x16x32_bf16 v[36:39], v[136:139], v[80:83], v[36:39]
v_mfma_f32_16x16x32_bf16 v[60:63], v[140:143], v[84:87], v[60:63]
v_mfma_f32_16x16x32_bf16 v[40:43], v[140:143], v[80:83], v[40:43]
v_mfma_f32_16x16x32_bf16 v[64:67], v[144:147], v[84:87], v[64:67]
v_mfma_f32_16x16x32_bf16 v[44:47], v[144:147], v[80:83], v[44:47]
v_mfma_f32_16x16x32_bf16 v[68:71], v[148:151], v[84:87], v[68:71]
v_mfma_f32_16x16x32_bf16 v[48:51], v[148:151], v[80:83], v[48:51]
s_waitcnt vmcnt(6)
v_mfma_f32_16x16x32_bf16 v[52:55], v[152:155], v[92:95], v[52:55]
v_mfma_f32_16x16x32_bf16 v[32:35], v[152:155], v[88:91], v[32:35]
v_mfma_f32_16x16x32_bf16 v[56:59], v[156:159], v[92:95], v[56:59]
v_mfma_f32_16x16x32_bf16 v[36:39], v[156:159], v[88:91], v[36:39]
v_mfma_f32_16x16x32_bf16 v[60:63], v[160:163], v[92:95], v[60:63]
v_mfma_f32_16x16x32_bf16 v[40:43], v[160:163], v[88:91], v[40:43]
v_mfma_f32_16x16x32_bf16 v[64:67], v[164:167], v[92:95], v[64:67]
v_mfma_f32_16x16x32_bf16 v[44:47], v[164:167], v[88:91], v[44:47]
v_mfma_f32_16x16x32_bf16 v[68:71], v[168:171], v[92:95], v[68:71]
v_mfma_f32_16x16x32_bf16 v[48:51], v[168:171], v[88:91], v[48:51]
s_waitcnt vmcnt(4)
v_mfma_f32_16x16x32_bf16 v[52:55], v[172:175], v[100:103], v[52:55]
v_mfma_f32_16x16x32_bf16 v[32:35], v[172:175], v[96:99], v[32:35]
v_mfma_f32_16x16x32_bf16 v[56:59], v[176:179], v[100:103], v[56:59]
v_mfma_f32_16x16x32_bf16 v[36:39], v[176:179], v[96:99], v[36:39]
v_mfma_f32_16x16x32_bf16 v[60:63], v[180:183], v[100:103], v[60:63]
v_mfma_f32_16x16x32_bf16 v[40:43], v[180:183], v[96:99], v[40:43]
v_mfma_f32_16x16x32_bf16 v[64:67], v[184:187], v[100:103], v[64:67]
v_mfma_f32_16x16x32_bf16 v[44:47], v[184:187], v[96:99], v[44:47]
v_mfma_f32_16x16x32_bf16 v[68:71], v[188:191], v[100:103], v[68:71]
v_mfma_f32_16x16x32_bf16 v[48:51], v[188:191], v[96:99], v[48:51]
s_waitcnt vmcnt(0)
v_mfma_f32_16x16x32_bf16 v[52:55], v[192:195], v[108:111], v[52:55]
v_mfma_f32_16x16x32_bf16 v[32:35], v[192:195], v[104:107], v[32:35]
v_mfma_f32_16x16x32_bf16 v[56:59], v[196:199], v[108:111], v[56:59]
v_mfma_f32_16x16x32_bf16 v[36:39], v[196:199], v[104:107], v[36:39]
v_mfma_f32_16x16x32_bf16 v[60:63], v[200:203], v[108:111], v[60:63]
v_mfma_f32_16x16x32_bf16 v[40:43], v[200:203], v[104:107], v[40:43]
v_mfma_f32_16x16x32_bf16 v[64:67], v[204:207], v[108:111], v[64:67]
v_mfma_f32_16x16x32_bf16 v[44:47], v[204:207], v[104:107], v[44:47]
v_mfma_f32_16x16x32_bf16 v[68:71], v[208:211], v[108:111], v[68:71]
v_mfma_f32_16x16x32_bf16 v[48:51], v[208:211], v[104:107], v[48:51]
s_nop 3
v_xor_b32 v16, 1024, v215
ds_write_b128 v212, v[52:55] offset:0
ds_write_b128 v212, v[56:59] offset:1024
ds_write_b128 v214, v[60:63] offset:0
ds_write_b128 v214, v[64:67] offset:1024
ds_write_b128 v213, v[40:43] offset:0
ds_write_b128 v213, v[44:47] offset:1024
ds_write_b128 v16, v[68:71]
ds_write_b128 v215, v[48:51]
s_mul_i32 s50, s47, 6144
s_add_u32 s50, s50, 81920
v_add_u32 v16, s50, v2
s_and_b32 s51, s47, 1
s_lshl_b32 s51, s51, 10
s_lshl_b32 s52, s71, 9
s_add_u32 s51, s51, s52
s_add_u32 s51, s51, 106496
v_and_b32 v17, 496, v2
v_add_u32 v17, s51, v17
s_waitcnt lgkmcnt(0)
s_barrier
ds_read_b128 v[72:75], v16 offset:0
ds_read_b128 v[76:79], v16 offset:1024
ds_read_b128 v[80:83], v16 offset:2048
ds_read_b128 v[84:87], v16 offset:3072
ds_read_b128 v[88:91], v16 offset:4096
ds_read_b128 v[92:95], v16 offset:5120
ds_read_b128 v[96:99], v17 offset:0
ds_read_b128 v[100:103], v17 offset:2048
ds_read_b128 v[104:107], v17 offset:4096
ds_read_b128 v[108:111], v17 offset:6144
s_waitcnt lgkmcnt(8)
v_pk_add_f32 v[32:33], v[32:33], v[72:73]
v_pk_add_f32 v[34:35], v[34:35], v[74:75]
v_pk_add_f32 v[36:37], v[36:37], v[76:77]
v_pk_add_f32 v[38:39], v[38:39], v[78:79]
s_waitcnt lgkmcnt(6)
v_pk_add_f32 v[32:33], v[32:33], v[80:81]
v_pk_add_f32 v[34:35], v[34:35], v[82:83]
v_pk_add_f32 v[36:37], v[36:37], v[84:85]
v_pk_add_f32 v[38:39], v[38:39], v[86:87]
s_waitcnt lgkmcnt(4)
v_pk_add_f32 v[32:33], v[32:33], v[88:89]
v_pk_add_f32 v[34:35], v[34:35], v[90:91]
v_pk_add_f32 v[36:37], v[36:37], v[92:93]
v_pk_add_f32 v[38:39], v[38:39], v[94:95]
s_waitcnt lgkmcnt(2)
v_pk_add_f32 v[96:97], v[96:97], v[100:101]
v_pk_add_f32 v[98:99], v[98:99], v[102:103]
s_waitcnt lgkmcnt(0)
v_pk_add_f32 v[104:105], v[104:105], v[108:109]
v_pk_add_f32 v[106:107], v[106:107], v[110:111]
v_pk_add_f32 v[96:97], v[96:97], v[104:105]
v_pk_add_f32 v[98:99], v[98:99], v[106:107]
v_lshlrev_b32 v52, 16, v26
v_and_b32 v53, 0xffff0000, v26
v_pk_add_f32 v[32:33], v[32:33], v[52:53]
v_lshlrev_b32 v52, 16, v27
v_and_b32 v53, 0xffff0000, v27
v_pk_add_f32 v[34:35], v[34:35], v[52:53]
v_lshlrev_b32 v52, 16, v28
v_and_b32 v53, 0xffff0000, v28
v_pk_add_f32 v[36:37], v[36:37], v[52:53]
v_lshlrev_b32 v52, 16, v29
v_and_b32 v53, 0xffff0000, v29
v_pk_add_f32 v[38:39], v[38:39], v[52:53]
v_lshlrev_b32 v52, 16, v30
v_and_b32 v53, 0xffff0000, v30
v_pk_add_f32 v[96:97], v[96:97], v[52:53]
v_lshlrev_b32 v52, 16, v31
v_and_b32 v53, 0xffff0000, v31
v_pk_add_f32 v[98:99], v[98:99], v[52:53]
v_cvt_pk_bf16_f32 v4, v32, v33
v_cvt_pk_bf16_f32 v5, v34, v35
v_cvt_pk_bf16_f32 v6, v36, v37
v_cvt_pk_bf16_f32 v7, v38, v39
v_cvt_pk_bf16_f32 v8, v96, v97
v_cvt_pk_bf16_f32 v9, v98, v99
buffer_store_dwordx4 v[4:7], v23, s[12:15], 0 offen sc1
buffer_store_dwordx2 v[8:9], v24, s[12:15], 0 offen sc1
v_cmp_gt_u32 vcc, 32, v0
v_mov_b32 v16, 0
v_mov_b32 v17, 0
v_mov_b32 v18, 0x3f803f80
v_cndmask_b32 v56, 0, v8, vcc
v_cndmask_b32 v57, 0, v9, vcc
v_dot2c_f32_bf16 v16, v4, v18
v_dot2c_f32_bf16 v17, v4, v4
v_dot2c_f32_bf16 v16, v5, v18
v_dot2c_f32_bf16 v17, v5, v5
v_dot2c_f32_bf16 v16, v6, v18
v_dot2c_f32_bf16 v17, v6, v6
v_dot2c_f32_bf16 v16, v7, v18
v_dot2c_f32_bf16 v17, v7, v7
v_dot2c_f32_bf16 v16, v56, v18
v_dot2c_f32_bf16 v17, v56, v56
v_dot2c_f32_bf16 v16, v57, v18
v_dot2c_f32_bf16 v17, v57, v57
s_nop 2
v_permlane32_swap_b32 v16, v17
v_add_f32 v16, v16, v17
v_mov_b32 v17, v16
s_nop 1
v_permlane16_swap_b32 v16, v17
v_add_f32 v16, v16, v17
buffer_store_dword v16, v25, s[12:15], 0 offen sc1
s_waitcnt vmcnt(0)
v_mov_b32 v18, s44
v_mov_b32 v19, s45
s_mov_b64 exec, 0xffff
buffer_store_dwordx2 v[18:19], v3, s[12:15], s77 offen sc1
s_mov_b64 exec, -1
v_bfe_u32 v40, v0, 2, 1
v_bfe_u32 v41, v0, 4, 1
v_xor_b32 v40, v40, v41
v_and_b32 v41, 3, v0
v_or_b32 v40, v40, v41
v_cmp_eq_u32 vcc, 0, v40
v_mov_b32 v41, 0x3f803f80
s_nop 0
v_cndmask_b32 v32, 0, v41, vcc
v_mov_b32 v33, v32
v_mov_b32 v34, v32
v_mov_b32 v35, v32
s_lshl_b32 s50, s47, 7
s_add_u32 s50, s50, 147456
v_and_b32 v40, 31, v0
v_lshl_add_u32 v40, v40, 2, s50
v_mfma_f32_16x16x32_bf16 v[36:39], v[32:35], a[0:3], 0
v_mfma_f32_16x16x32_bf16 v[36:39], v[32:35], a[4:7], v[36:39]
v_mfma_f32_16x16x32_bf16 v[36:39], v[32:35], a[8:11], v[36:39]
v_mfma_f32_16x16x32_bf16 v[36:39], v[32:35], a[12:15], v[36:39]
v_mfma_f32_16x16x32_bf16 v[36:39], v[32:35], a[16:19], v[36:39]
v_mfma_f32_16x16x32_bf16 v[36:39], v[32:35], a[20:23], v[36:39]
v_mfma_f32_16x16x32_bf16 v[36:39], v[32:35], a[24:27], v[36:39]
v_mfma_f32_16x16x32_bf16 v[36:39], v[32:35], a[28:31], v[36:39]
v_mfma_f32_16x16x32_bf16 v[36:39], v[32:35], a[32:35], v[36:39]
v_mfma_f32_16x16x32_bf16 v[36:39], v[32:35], a[36:39], v[36:39]
v_mfma_f32_16x16x32_bf16 v[36:39], v[32:35], a[40:43], v[36:39]
v_mfma_f32_16x16x32_bf16 v[36:39], v[32:35], a[44:47], v[36:39]
v_mfma_f32_16x16x32_bf16 v[36:39], v[32:35], a[48:51], v[36:39]
v_mfma_f32_16x16x32_bf16 v[36:39], v[32:35], a[52:55], v[36:39]
v_mfma_f32_16x16x32_bf16 v[36:39], v[32:35], a[56:59], v[36:39]
v_mfma_f32_16x16x32_bf16 v[36:39], v[32:35], a[60:63], v[36:39]
v_mfma_f32_16x16x32_bf16 v[36:39], v[32:35], a[64:67], v[36:39]
v_mfma_f32_16x16x32_bf16 v[36:39], v[32:35], a[68:71], v[36:39]
v_mfma_f32_16x16x32_bf16 v[36:39], v[32:35], a[72:75], v[36:39]
v_mfma_f32_16x16x32_bf16 v[36:39], v[32:35], a[76:79], v[36:39]
v_mfma_f32_16x16x32_bf16 v[36:39], v[32:35], a[80:83], v[36:39]
v_mfma_f32_16x16x32_bf16 v[36:39], v[32:35], a[84:87], v[36:39]
v_mfma_f32_16x16x32_bf16 v[36:39], v[32:35], a[88:91], v[36:39]
v_mfma_f32_16x16x32_bf16 v[36:39], v[32:35], a[92:95], v[36:39]
v_mfma_f32_16x16x32_bf16 v[36:39], v[32:35], a[96:99], v[36:39]
v_mfma_f32_16x16x32_bf16 v[36:39], v[32:35], a[100:103], v[36:39]
v_mfma_f32_16x16x32_bf16 v[36:39], v[32:35], a[104:107], v[36:39]
v_mfma_f32_16x16x32_bf16 v[36:39], v[32:35], a[108:111], v[36:39]
v_mfma_f32_16x16x32_bf16 v[36:39], v[32:35], a[112:115], v[36:39]
v_mfma_f32_16x16x32_bf16 v[36:39], v[32:35], a[116:119], v[36:39]
v_mfma_f32_16x16x32_bf16 v[36:39], v[32:35], a[120:123], v[36:39]
v_mfma_f32_16x16x32_bf16 v[36:39], v[32:35], a[124:127], v[36:39]
v_mfma_f32_16x16x32_bf16 v[36:39], v[32:35], a[128:131], v[36:39]
v_mfma_f32_16x16x32_bf16 v[36:39], v[32:35], a[132:135], v[36:39]
v_mfma_f32_16x16x32_bf16 v[36:39], v[32:35], a[136:139], v[36:39]
v_mfma_f32_16x16x32_bf16 v[36:39], v[32:35], a[140:143], v[36:39]
v_mfma_f32_16x16x32_bf16 v[36:39], v[32:35], a[144:147], v[36:39]
v_mfma_f32_16x16x32_bf16 v[36:39], v[32:35], a[148:151], v[36:39]
v_mfma_f32_16x16x32_bf16 v[36:39], v[32:35], a[152:155], v[36:39]
v_mfma_f32_16x16x32_bf16 v[36:39], v[32:35], a[156:159], v[36:39]
s_nop 7
ds_write_b32 v40, v36
s_mov_b32 s54, 163840
buffer_load_dwordx4 v[26:29], v20, s[4:7], s54 offen
buffer_load_dwordx2 v[30:31], v21, s[4:7], s54 offen
s_xor_b32 s50, s47, 1
s_mul_i32 s50, s50, 6144
s_add_u32 s50, s50, 114688
v_add_u32 v212, s50, v2
s_xor_b32 s50, s47, 2
s_mul_i32 s50, s50, 6144
s_add_u32 s50, s50, 116736
v_add_u32 v213, s50, v2
s_xor_b32 s50, s47, 3
s_mul_i32 s50, s50, 6144
s_add_u32 s50, s50, 118784
v_add_u32 v214, s50, v2
s_lshl_b32 s50, s47, 11
s_and_b32 s51, s47, 1
s_lshl_b32 s51, s51, 10
s_add_u32 s50, s50, s51
s_add_u32 s50, s50, 139264
v_add_u32 v215, s50, v2
s_lshl_b32 s50, s48, 2
s_lshl_b32 s51, s91, 1
s_add_u32 s50, s50, s51
s_add_u32 s50, s50, s90
s_lshl_b32 s50, s50, 2
s_add_u32 s50, s50, s47
s_lshl_b32 s50, s50, 10
s_add_u32 s50, s50, 393216
v_and_b32 v16, 7, v0
v_lshlrev_b32 v16, 7, v16
s_add_u32 s51, s70, s90
s_lshl_b32 s51, s51, 1
s_and_b32 s52, s47, 1
s_xor_b32 s52, s52, 0
s_add_u32 s51, s51, s52
s_add_u32 s51, s51, 8
s_lshl_b32 s52, s51, 14
s_lshl_b32 s53, s47, 12
)ASM"
      R"ASM(
s_add_u32 s52, s52, s53
s_add_u32 s73, s52, 10485760
s_lshl_b32 s52, s51, 12
s_lshl_b32 s53, s47, 10
s_add_u32 s52, s52, s53
s_add_u32 s75, s52, 12582912
s_add_u32 s51, s70, s90
s_lshl_b32 s51, s51, 1
s_and_b32 s52, s47, 1
s_xor_b32 s52, s52, 1
s_add_u32 s51, s51, s52
s_add_u32 s51, s51, 8
s_lshl_b32 s52, s51, 14
s_lshl_b32 s53, s47, 12
s_add_u32 s52, s52, s53
s_add_u32 s74, s52, 10485760
s_lshl_b32 s52, s51, 12
s_lshl_b32 s53, s47, 10
s_add_u32 s52, s52, s53
s_add_u32 s76, s52, 12582912
s_lshl_b32 s51, s72, 4
s_and_b32 s52, s49, 15
s_add_u32 s51, s51, s52
s_lshl_b32 s51, s51, 2
s_add_u32 s51, s51, s47
s_lshl_b32 s51, s51, 7
s_add_u32 s77, s51, 655360
.Lmk_poll_38:
buffer_load_dwordx2 v[18:19], v16, s[12:15], s50 offen sc1
s_waitcnt vmcnt(0)
v_cmp_ne_u64 vcc, s[44:45], v[18:19]
s_nop 0
s_cbranch_vccz .Lmk_polld_39
s_sub_u32 s46, s46, 1
s_cbranch_scc0 .Lmk_poll_38
s_mov_b32 s46, 0
.Lmk_polld_39:
buffer_load_dwordx4 v[72:75], v2, s[12:15], s73 offen offset:0 sc1
buffer_load_dwordx4 v[76:79], v2, s[12:15], s74 offen offset:0 sc1
buffer_load_dwordx4 v[80:83], v2, s[12:15], s73 offen offset:1024 sc1
buffer_load_dwordx4 v[84:87], v2, s[12:15], s74 offen offset:1024 sc1
buffer_load_dwordx4 v[88:91], v2, s[12:15], s73 offen offset:2048 sc1
buffer_load_dwordx4 v[92:95], v2, s[12:15], s74 offen offset:2048 sc1
buffer_load_dwordx4 v[96:99], v2, s[12:15], s73 offen offset:3072 sc1
buffer_load_dwordx4 v[100:103], v2, s[12:15], s74 offen offset:3072 sc1
buffer_load_dwordx2 v[104:105], v22, s[12:15], s75 offen offset:0 sc1
buffer_load_dwordx2 v[106:107], v22, s[12:15], s75 offen offset:128 sc1
buffer_load_dwordx2 v[108:109], v22, s[12:15], s76 offen offset:0 sc1
buffer_load_dwordx2 v[110:111], v22, s[12:15], s76 offen offset:128 sc1
s_waitcnt vmcnt(10)
v_mfma_f32_16x16x32_bf16 v[52:55], v[112:115], v[76:79], 0
v_mfma_f32_16x16x32_bf16 v[32:35], v[112:115], v[72:75], 0
v_mfma_f32_16x16x32_bf16 v[56:59], v[116:119], v[76:79], 0
v_mfma_f32_16x16x32_bf16 v[36:39], v[116:119], v[72:75], 0
v_mfma_f32_16x16x32_bf16 v[60:63], v[120:123], v[76:79], 0
v_mfma_f32_16x16x32_bf16 v[40:43], v[120:123], v[72:75], 0
v_mfma_f32_16x16x32_bf16 v[64:67], v[124:127], v[76:79], 0
v_mfma_f32_16x16x32_bf16 v[44:47], v[124:127], v[72:75], 0
v_mfma_f32_16x16x32_bf16 v[68:71], v[128:131], v[76:79], 0
v_mfma_f32_16x16x32_bf16 v[48:51], v[128:131], v[72:75], 0
s_waitcnt vmcnt(8)
v_mfma_f32_16x16x32_bf16 v[52:55], v[132:135], v[84:87], v[52:55]
v_mfma_f32_16x16x32_bf16 v[32:35], v[132:135], v[80:83], v[32:35]
v_mfma_f32_16x16x32_bf16 v[56:59], v[136:139], v[84:87], v[56:59]
v_mfma_f32_16x16x32_bf16 v[36:39], v[136:139], v[80:83], v[36:39]
v_mfma_f32_16x16x32_bf16 v[60:63], v[140:143], v[84:87], v[60:63]
v_mfma_f32_16x16x32_bf16 v[40:43], v[140:143], v[80:83], v[40:43]
v_mfma_f32_16x16x32_bf16 v[64:67], v[144:147], v[84:87], v[64:67]
v_mfma_f32_16x16x32_bf16 v[44:47], v[144:147], v[80:83], v[44:47]
v_mfma_f32_16x16x32_bf16 v[68:71], v[148:151], v[84:87], v[68:71]
v_mfma_f32_16x16x32_bf16 v[48:51], v[148:151], v[80:83], v[48:51]
s_waitcnt vmcnt(6)
v_mfma_f32_16x16x32_bf16 v[52:55], v[152:155], v[92:95], v[52:55]
v_mfma_f32_16x16x32_bf16 v[32:35], v[152:155], v[88:91], v[32:35]
v_mfma_f32_16x16x32_bf16 v[56:59], v[156:159], v[92:95], v[56:59]
v_mfma_f32_16x16x32_bf16 v[36:39], v[156:159], v[88:91], v[36:39]
v_mfma_f32_16x16x32_bf16 v[60:63], v[160:163], v[92:95], v[60:63]
v_mfma_f32_16x16x32_bf16 v[40:43], v[160:163], v[88:91], v[40:43]
v_mfma_f32_16x16x32_bf16 v[64:67], v[164:167], v[92:95], v[64:67]
v_mfma_f32_16x16x32_bf16 v[44:47], v[164:167], v[88:91], v[44:47]
v_mfma_f32_16x16x32_bf16 v[68:71], v[168:171], v[92:95], v[68:71]
v_mfma_f32_16x16x32_bf16 v[48:51], v[168:171], v[88:91], v[48:51]
s_waitcnt vmcnt(4)
v_mfma_f32_16x16x32_bf16 v[52:55], v[172:175], v[100:103], v[52:55]
v_mfma_f32_16x16x32_bf16 v[32:35], v[172:175], v[96:99], v[32:35]
v_mfma_f32_16x16x32_bf16 v[56:59], v[176:179], v[100:103], v[56:59]
v_mfma_f32_16x16x32_bf16 v[36:39], v[176:179], v[96:99], v[36:39]
v_mfma_f32_16x16x32_bf16 v[60:63], v[180:183], v[100:103], v[60:63]
v_mfma_f32_16x16x32_bf16 v[40:43], v[180:183], v[96:99], v[40:43]
v_mfma_f32_16x16x32_bf16 v[64:67], v[184:187], v[100:103], v[64:67]
v_mfma_f32_16x16x32_bf16 v[44:47], v[184:187], v[96:99], v[44:47]
v_mfma_f32_16x16x32_bf16 v[68:71], v[188:191], v[100:103], v[68:71]
v_mfma_f32_16x16x32_bf16 v[48:51], v[188:191], v[96:99], v[48:51]
s_waitcnt vmcnt(0)
v_mfma_f32_16x16x32_bf16 v[52:55], v[192:195], v[108:111], v[52:55]
v_mfma_f32_16x16x32_bf16 v[32:35], v[192:195], v[104:107], v[32:35]
v_mfma_f32_16x16x32_bf16 v[56:59], v[196:199], v[108:111], v[56:59]
v_mfma_f32_16x16x32_bf16 v[36:39], v[196:199], v[104:107], v[36:39]
v_mfma_f32_16x16x32_bf16 v[60:63], v[200:203], v[108:111], v[60:63]
v_mfma_f32_16x16x32_bf16 v[40:43], v[200:203], v[104:107], v[40:43]
v_mfma_f32_16x16x32_bf16 v[64:67], v[204:207], v[108:111], v[64:67]
v_mfma_f32_16x16x32_bf16 v[44:47], v[204:207], v[104:107], v[44:47]
v_mfma_f32_16x16x32_bf16 v[68:71], v[208:211], v[108:111], v[68:71]
v_mfma_f32_16x16x32_bf16 v[48:51], v[208:211], v[104:107], v[48:51]
s_nop 3
v_xor_b32 v16, 1024, v215
ds_write_b128 v212, v[52:55] offset:0
ds_write_b128 v212, v[56:59] offset:1024
ds_write_b128 v214, v[60:63] offset:0
ds_write_b128 v214, v[64:67] offset:1024
ds_write_b128 v213, v[40:43] offset:0
ds_write_b128 v213, v[44:47] offset:1024
ds_write_b128 v16, v[68:71]
ds_write_b128 v215, v[48:51]
s_mul_i32 s50, s47, 6144
s_add_u32 s50, s50, 114688
v_add_u32 v16, s50, v2
s_and_b32 s51, s47, 1
s_lshl_b32 s51, s51, 10
s_lshl_b32 s52, s71, 9
s_add_u32 s51, s51, s52
s_add_u32 s51, s51, 139264
v_and_b32 v17, 496, v2
v_add_u32 v17, s51, v17
s_waitcnt lgkmcnt(0)
s_barrier
ds_read_b128 v[72:75], v16 offset:0
ds_read_b128 v[76:79], v16 offset:1024
ds_read_b128 v[80:83], v16 offset:2048
ds_read_b128 v[84:87], v16 offset:3072
ds_read_b128 v[88:91], v16 offset:4096
ds_read_b128 v[92:95], v16 offset:5120
ds_read_b128 v[96:99], v17 offset:0
ds_read_b128 v[100:103], v17 offset:2048
ds_read_b128 v[104:107], v17 offset:4096
ds_read_b128 v[108:111], v17 offset:6144
s_waitcnt lgkmcnt(8)
v_pk_add_f32 v[32:33], v[32:33], v[72:73]
v_pk_add_f32 v[34:35], v[34:35], v[74:75]
v_pk_add_f32 v[36:37], v[36:37], v[76:77]
v_pk_add_f32 v[38:39], v[38:39], v[78:79]
s_waitcnt lgkmcnt(6)
v_pk_add_f32 v[32:33], v[32:33], v[80:81]
v_pk_add_f32 v[34:35], v[34:35], v[82:83]
v_pk_add_f32 v[36:37], v[36:37], v[84:85]
v_pk_add_f32 v[38:39], v[38:39], v[86:87]
s_waitcnt lgkmcnt(4)
v_pk_add_f32 v[32:33], v[32:33], v[88:89]
v_pk_add_f32 v[34:35], v[34:35], v[90:91]
v_pk_add_f32 v[36:37], v[36:37], v[92:93]
v_pk_add_f32 v[38:39], v[38:39], v[94:95]
s_waitcnt lgkmcnt(2)
v_pk_add_f32 v[96:97], v[96:97], v[100:101]
v_pk_add_f32 v[98:99], v[98:99], v[102:103]
s_waitcnt lgkmcnt(0)
v_pk_add_f32 v[104:105], v[104:105], v[108:109]
v_pk_add_f32 v[106:107], v[106:107], v[110:111]
v_pk_add_f32 v[96:97], v[96:97], v[104:105]
v_pk_add_f32 v[98:99], v[98:99], v[106:107]
v_lshlrev_b32 v52, 16, v26
v_and_b32 v53, 0xffff0000, v26
v_pk_add_f32 v[32:33], v[32:33], v[52:53]
v_lshlrev_b32 v52, 16, v27
v_and_b32 v53, 0xffff0000, v27
v_pk_add_f32 v[34:35], v[34:35], v[52:53]
v_lshlrev_b32 v52, 16, v28
v_and_b32 v53, 0xffff0000, v28
v_pk_add_f32 v[36:37], v[36:37], v[52:53]
v_lshlrev_b32 v52, 16, v29
v_and_b32 v53, 0xffff0000, v29
v_pk_add_f32 v[38:39], v[38:39], v[52:53]
v_lshlrev_b32 v52, 16, v30
v_and_b32 v53, 0xffff0000, v30
v_pk_add_f32 v[96:97], v[96:97], v[52:53]
v_lshlrev_b32 v52, 16, v31
v_and_b32 v53, 0xffff0000, v31
v_pk_add_f32 v[98:99], v[98:99], v[52:53]
v_cvt_pk_bf16_f32 v10, v32, v33
v_cvt_pk_bf16_f32 v11, v34, v35
v_cvt_pk_bf16_f32 v12, v36, v37
v_cvt_pk_bf16_f32 v13, v38, v39
v_cvt_pk_bf16_f32 v14, v96, v97
v_cvt_pk_bf16_f32 v15, v98, v99
s_mov_b32 s54, 163840
buffer_store_dwordx4 v[10:13], v23, s[12:15], s54 offen sc1
buffer_store_dwordx2 v[14:15], v24, s[12:15], s54 offen sc1
v_cmp_gt_u32 vcc, 32, v0
v_mov_b32 v16, 0
v_mov_b32 v17, 0
v_mov_b32 v18, 0x3f803f80
v_cndmask_b32 v56, 0, v14, vcc
v_cndmask_b32 v57, 0, v15, vcc
v_dot2c_f32_bf16 v16, v10, v18
v_dot2c_f32_bf16 v17, v10, v10
v_dot2c_f32_bf16 v16, v11, v18
v_dot2c_f32_bf16 v17, v11, v11
v_dot2c_f32_bf16 v16, v12, v18
v_dot2c_f32_bf16 v17, v12, v12
v_dot2c_f32_bf16 v16, v13, v18
v_dot2c_f32_bf16 v17, v13, v13
v_dot2c_f32_bf16 v16, v56, v18
v_dot2c_f32_bf16 v17, v56, v56
v_dot2c_f32_bf16 v16, v57, v18
v_dot2c_f32_bf16 v17, v57, v57
s_nop 2
v_permlane32_swap_b32 v16, v17
v_add_f32 v16, v16, v17
v_mov_b32 v17, v16
s_nop 1
v_permlane16_swap_b32 v16, v17
v_add_f32 v16, v16, v17
s_mov_b32 s54, 16384
buffer_store_dword v16, v25, s[12:15], s54 offen sc1
s_waitcnt vmcnt(0)
v_mov_b32 v18, s44
v_mov_b32 v19, s45
s_mov_b64 exec, 0xffff
buffer_store_dwordx2 v[18:19], v3, s[12:15], s77 offen sc1
s_mov_b64 exec, -1
s_and_b32 s50, s49, 15
s_cmp_ge_u32 s50, 14
s_cbranch_scc1 .Lmk_nof1_40
v_lshrrev_b32 v16, 5, v0
s_lshl_b32 s50, s92, 6
v_lshl_add_u32 v17, v16, 3, s50
buffer_load_dwordx2 v[120:121], v17, s[32:35], 0 offen offset:0
buffer_load_dwordx2 v[122:123], v17, s[32:35], 0 offen offset:16
buffer_load_dwordx2 v[124:125], v17, s[32:35], 0 offen offset:32
buffer_load_dwordx2 v[126:127], v17, s[32:35], 0 offen offset:48
s_lshl_b32 s50, s47, 7
s_add_u32 s50, s50, 147456
v_lshl_add_u32 v17, v16, 4, s50
ds_read_b128 v[96:99], v17 offset:0
ds_read_b128 v[100:103], v17 offset:32
ds_read_b128 v[104:107], v17 offset:64
ds_read_b128 v[108:111], v17 offset:96
v_mov_b32 v30, 0xbdd2d3e8
v_bfe_u32 v17, v0, 4, 1
v_mul_u32_u24 v17, 55296, v17
v_lshl_add_u32 v17, v16, 8, v17
v_and_b32 v18, 15, v0
v_lshl_add_u32 v28, v18, 4, v17
v_lshrrev_b32 v17, 4, v0
v_lshlrev_b32 v17, 9, v17
v_lshl_add_u32 v29, v18, 3, v17
s_waitcnt vmcnt(0) lgkmcnt(0)
v_lshlrev_b32 v80, 16, v120
v_and_b32 v81, 0xffff0000, v120
v_lshlrev_b32 v82, 16, v121
v_and_b32 v83, 0xffff0000, v121
v_lshlrev_b32 v84, 16, v122
v_and_b32 v85, 0xffff0000, v122
v_lshlrev_b32 v86, 16, v123
v_and_b32 v87, 0xffff0000, v123
v_lshlrev_b32 v88, 16, v124
v_and_b32 v89, 0xffff0000, v124
v_lshlrev_b32 v90, 16, v125
v_and_b32 v91, 0xffff0000, v125
v_lshlrev_b32 v92, 16, v126
v_and_b32 v93, 0xffff0000, v126
v_lshlrev_b32 v94, 16, v127
v_and_b32 v95, 0xffff0000, v127
v_bfe_u32 v16, v0, 3, 1
v_and_b32 v17, 7, v0
v_lshl_add_u32 v16, v16, 5, v17
v_lshlrev_b32 v16, 7, v16
s_lshl_b32 s50, s72, 3
s_add_u32 s50, s50, s47
s_lshl_b32 s50, s50, 10
s_add_u32 s50, s50, 524288
s_add_u32 s51, s70, 0
s_mul_i32 s52, s51, 40960
s_mul_i32 s53, s47, 10240
s_add_u32 s52, s52, s53
s_add_u32 s73, s52, 13631488
s_add_u32 s74, s53, 0
s_mul_i32 s52, s51, 110592
s_lshl_b32 s53, s92, 10
s_add_u32 s52, s52, s53
s_add_u32 s75, s52, 16777216
s_cmp_ge_u32 s92, 54
s_cselect_b32 s75, 0x40000000, s75
s_add_u32 s76, s75, 110592
s_lshl_b32 s52, s51, 1
s_add_u32 s52, s52, s47
s_lshl_b32 s52, s52, 11
s_add_u32 s78, s52, 16252928
s_mul_i32 s52, s72, 108
s_add_u32 s52, s52, s92
s_lshl_b32 s52, s52, 7
s_add_u32 s77, s52, 786432
s_cmp_ge_u32 s92, 54
s_cselect_b32 s77, 0x40000000, s77
v_add_u32 v26, 0, v2
v_add_u32 v27, 40960, v2
s_lshl_b32 s52, s47, 7
s_add_u32 s52, s52, 148480
v_and_b32 v17, 15, v0
v_lshl_add_u32 v31, v17, 3, s52
v_lshrrev_b32 v17, 5, v0
v_lshl_add_u32 v31, v17, 2, v31
.Lmk_poll_42:
buffer_load_dwordx2 v[18:19], v16, s[12:15], s50 offen sc1
s_waitcnt vmcnt(0)
v_cmp_ne_u64 vcc, s[44:45], v[18:19]
s_nop 0
s_cbranch_vccz .Lmk_polld_43
s_sub_u32 s46, s46, 1
s_cbranch_scc0 .Lmk_poll_42
s_mov_b32 s46, 0
.Lmk_polld_43:
s_add_u32 m0, s74, 0
s_add_u32 s55, s73, 0
buffer_load_dwordx4 v2, s[12:15], s55 offen offset:0 sc1 lds
buffer_load_dwordx4 v2, s[12:15], s55 offen offset:1024 sc1 lds
buffer_load_dwordx4 v2, s[12:15], s55 offen offset:2048 sc1 lds
buffer_load_dwordx4 v2, s[12:15], s55 offen offset:3072 sc1 lds
s_add_u32 m0, s74, 4096
s_add_u32 s55, s73, 4096
buffer_load_dwordx4 v2, s[12:15], s55 offen offset:0 sc1 lds
buffer_load_dwordx4 v2, s[12:15], s55 offen offset:1024 sc1 lds
buffer_load_dwordx4 v2, s[12:15], s55 offen offset:2048 sc1 lds
buffer_load_dwordx4 v2, s[12:15], s55 offen offset:3072 sc1 lds
s_add_u32 m0, s74, 8192
s_add_u32 s55, s73, 8192
buffer_load_dwordx4 v2, s[12:15], s55 offen offset:0 sc1 lds
buffer_load_dwordx4 v2, s[12:15], s55 offen offset:1024 sc1 lds
s_waitcnt vmcnt(8)
s_barrier
ds_read_b128 v[64:67], v26 offset:0
ds_read_b128 v[68:71], v26 offset:10240
ds_read_b128 v[72:75], v26 offset:20480
s_waitcnt lgkmcnt(1)
v_mfma_f32_32x32x16_bf16 v[32:47], a[0:3], v[64:67], 0
ds_read_b128 v[76:79], v26 offset:30720
buffer_load_dwordx2 v[120:121], v29, s[12:15], s78 offen offset:0 sc1
v_mfma_f32_32x32x16_bf16 v[32:47], a[40:43], v[68:71], v[32:47]
ds_read_b128 v[64:67], v26 offset:1024
buffer_load_dwordx2 v[122:123], v29, s[12:15], s78 offen offset:128 sc1
s_waitcnt lgkmcnt(1)
v_mfma_f32_32x32x16_bf16 v[32:47], a[80:83], v[72:75], v[32:47]
ds_read_b128 v[68:71], v26 offset:11264
buffer_load_dwordx2 v[124:125], v29, s[12:15], s78 offen offset:256 sc1
v_mfma_f32_32x32x16_bf16 v[32:47], a[120:123], v[76:79], v[32:47]
ds_read_b128 v[72:75], v26 offset:21504
buffer_load_dwordx2 v[126:127], v29, s[12:15], s78 offen offset:384 sc1
s_waitcnt lgkmcnt(1)
v_mfma_f32_32x32x16_bf16 v[32:47], a[4:7], v[64:67], v[32:47]
ds_read_b128 v[76:79], v26 offset:31744
s_add_u32 m0, s74, 40960
s_add_u32 s55, s73, 40960
buffer_load_dwordx4 v2, s[12:15], s55 offen offset:0 sc1 lds
v_mfma_f32_32x32x16_bf16 v[32:47], a[44:47], v[68:71], v[32:47]
s_waitcnt vmcnt(9)
s_barrier
ds_read_b128 v[64:67], v26 offset:2048
s_waitcnt lgkmcnt(1)
v_mfma_f32_32x32x16_bf16 v[32:47], a[84:87], v[72:75], v[32:47]
ds_read_b128 v[68:71], v26 offset:12288
buffer_load_dwordx4 v2, s[12:15], s55 offen offset:1024 sc1 lds
v_mfma_f32_32x32x16_bf16 v[32:47], a[124:127], v[76:79], v[32:47]
ds_read_b128 v[72:75], v26 offset:22528
s_waitcnt lgkmcnt(1)
v_mfma_f32_32x32x16_bf16 v[32:47], a[8:11], v[64:67], v[32:47]
ds_read_b128 v[76:79], v26 offset:32768
buffer_load_dwordx4 v2, s[12:15], s55 offen offset:2048 sc1 lds
v_mfma_f32_32x32x16_bf16 v[32:47], a[48:51], v[68:71], v[32:47]
ds_read_b128 v[64:67], v26 offset:3072
s_waitcnt lgkmcnt(1)
v_mfma_f32_32x32x16_bf16 v[32:47], a[88:91], v[72:75], v[32:47]
ds_read_b128 v[68:71], v26 offset:13312
buffer_load_dwordx4 v2, s[12:15], s55 offen offset:3072 sc1 lds
v_mfma_f32_32x32x16_bf16 v[32:47], a[128:131], v[76:79], v[32:47]
ds_read_b128 v[72:75], v26 offset:23552
s_waitcnt lgkmcnt(1)
v_mfma_f32_32x32x16_bf16 v[32:47], a[12:15], v[64:67], v[32:47]
ds_read_b128 v[76:79], v26 offset:33792
s_add_u32 m0, s74, 45056
s_add_u32 s55, s73, 45056
buffer_load_dwordx4 v2, s[12:15], s55 offen offset:0 sc1 lds
v_mfma_f32_32x32x16_bf16 v[32:47], a[52:55], v[68:71], v[32:47]
ds_read_b128 v[64:67], v26 offset:4096
s_waitcnt lgkmcnt(1)
v_mfma_f32_32x32x16_bf16 v[32:47], a[92:95], v[72:75], v[32:47]
ds_read_b128 v[68:71], v26 offset:14336
buffer_load_dwordx4 v2, s[12:15], s55 offen offset:1024 sc1 lds
v_mfma_f32_32x32x16_bf16 v[32:47], a[132:135], v[76:79], v[32:47]
ds_read_b128 v[72:75], v26 offset:24576
s_waitcnt lgkmcnt(1)
v_mfma_f32_32x32x16_bf16 v[32:47], a[16:19], v[64:67], v[32:47]
ds_read_b128 v[76:79], v26 offset:34816
buffer_load_dwordx4 v2, s[12:15], s55 offen offset:2048 sc1 lds
v_mfma_f32_32x32x16_bf16 v[32:47], a[56:59], v[68:71], v[32:47]
ds_read_b128 v[64:67], v26 offset:5120
s_waitcnt lgkmcnt(1)
v_mfma_f32_32x32x16_bf16 v[32:47], a[96:99], v[72:75], v[32:47]
ds_read_b128 v[68:71], v26 offset:15360
buffer_load_dwordx4 v2, s[12:15], s55 offen offset:3072 sc1 lds
v_mfma_f32_32x32x16_bf16 v[32:47], a[136:139], v[76:79], v[32:47]
ds_read_b128 v[72:75], v26 offset:25600
)ASM"
      R"ASM(
s_waitcnt lgkmcnt(1)
v_mfma_f32_32x32x16_bf16 v[32:47], a[20:23], v[64:67], v[32:47]
ds_read_b128 v[76:79], v26 offset:35840
s_add_u32 m0, s74, 49152
s_add_u32 s55, s73, 49152
buffer_load_dwordx4 v2, s[12:15], s55 offen offset:0 sc1 lds
v_mfma_f32_32x32x16_bf16 v[32:47], a[60:63], v[68:71], v[32:47]
s_waitcnt vmcnt(13)
s_barrier
ds_read_b128 v[64:67], v26 offset:6144
s_waitcnt lgkmcnt(1)
v_mfma_f32_32x32x16_bf16 v[32:47], a[100:103], v[72:75], v[32:47]
ds_read_b128 v[68:71], v26 offset:16384
buffer_load_dwordx4 v2, s[12:15], s55 offen offset:1024 sc1 lds
v_mfma_f32_32x32x16_bf16 v[32:47], a[140:143], v[76:79], v[32:47]
ds_read_b128 v[72:75], v26 offset:26624
s_waitcnt lgkmcnt(1)
v_mfma_f32_32x32x16_bf16 v[32:47], a[24:27], v[64:67], v[32:47]
ds_read_b128 v[76:79], v26 offset:36864
buffer_load_dwordx4 v[128:131], v2, s[12:15], s65 offen offset:2048 sc1
v_mfma_f32_32x32x16_bf16 v[32:47], a[64:67], v[68:71], v[32:47]
ds_read_b128 v[64:67], v26 offset:7168
s_waitcnt lgkmcnt(1)
v_mfma_f32_32x32x16_bf16 v[32:47], a[104:107], v[72:75], v[32:47]
ds_read_b128 v[68:71], v26 offset:17408
buffer_load_dwordx4 v[132:135], v2, s[12:15], s65 offen offset:3072 sc1
s_waitcnt vmcnt(12)
v_add_f32 v120, v120, v124
v_add_f32 v121, v121, v125
v_add_f32 v122, v122, v126
v_add_f32 v123, v123, v127
v_mfma_f32_32x32x16_bf16 v[32:47], a[144:147], v[76:79], v[32:47]
ds_read_b128 v[72:75], v26 offset:27648
v_add_f32 v120, v120, v122
v_add_f32 v121, v121, v123
s_waitcnt lgkmcnt(1)
v_mfma_f32_32x32x16_bf16 v[32:47], a[28:31], v[64:67], v[32:47]
ds_read_b128 v[76:79], v26 offset:37888
s_add_u32 s65, s65, 4096
buffer_load_dwordx4 v[136:139], v2, s[12:15], s65 offen offset:0 sc1
v_mfma_f32_32x32x16_bf16 v[32:47], a[68:71], v[68:71], v[32:47]
ds_read_b128 v[64:67], v26 offset:8192
s_nop 2
v_permlane32_swap_b32 v120, v121
v_add_f32 v120, v120, v121
v_mov_b32 v121, v120
s_nop 1
v_permlane16_swap_b32 v120, v121
v_add_f32 v120, v120, v121
s_waitcnt lgkmcnt(1)
v_mfma_f32_32x32x16_bf16 v[32:47], a[108:111], v[72:75], v[32:47]
ds_read_b128 v[68:71], v26 offset:18432
buffer_load_dwordx4 v[140:143], v2, s[12:15], s65 offen offset:1024 sc1
v_mfma_f32_32x32x16_bf16 v[32:47], a[148:151], v[76:79], v[32:47]
ds_read_b128 v[72:75], v26 offset:28672
ds_write_b32 v31, v120
s_waitcnt lgkmcnt(2)
v_mfma_f32_32x32x16_bf16 v[32:47], a[32:35], v[64:67], v[32:47]
ds_read_b128 v[76:79], v26 offset:38912
buffer_load_dwordx4 v[144:147], v2, s[12:15], s65 offen offset:2048 sc1
v_mfma_f32_32x32x16_bf16 v[32:47], a[72:75], v[68:71], v[32:47]
ds_read_b128 v[64:67], v26 offset:9216
s_waitcnt lgkmcnt(2)
s_waitcnt lgkmcnt(1)
v_mfma_f32_32x32x16_bf16 v[32:47], a[112:115], v[72:75], v[32:47]
ds_read_b128 v[68:71], v26 offset:19456
buffer_load_dwordx4 v[148:151], v2, s[12:15], s65 offen offset:3072 sc1
v_mfma_f32_32x32x16_bf16 v[32:47], a[152:155], v[76:79], v[32:47]
ds_read_b128 v[72:75], v26 offset:29696
s_waitcnt lgkmcnt(1)
v_mfma_f32_32x32x16_bf16 v[32:47], a[36:39], v[64:67], v[32:47]
ds_read_b128 v[76:79], v26 offset:39936
s_add_u32 s65, s65, 4096
buffer_load_dwordx4 v[152:155], v2, s[12:15], s65 offen offset:0 sc1
v_mfma_f32_32x32x16_bf16 v[32:47], a[76:79], v[68:71], v[32:47]
s_waitcnt vmcnt(12)
s_barrier
ds_read_b128 v[64:67], v27 offset:0
v_and_b32 v16, 31, v0
v_lshlrev_b32 v16, 3, v16
v_add_u32 v16, 148480, v16
ds_read_b64 v[120:121], v16
ds_read_b64 v[122:123], v16 offset:256
s_waitcnt lgkmcnt(3)
v_mfma_f32_32x32x16_bf16 v[32:47], a[116:119], v[72:75], v[32:47]
ds_read_b128 v[68:71], v27 offset:10240
buffer_load_dwordx4 v[156:159], v2, s[12:15], s65 offen offset:1024 sc1
v_mfma_f32_32x32x16_bf16 v[32:47], a[156:159], v[76:79], v[32:47]
ds_read_b128 v[72:75], v27 offset:20480
s_waitcnt lgkmcnt(1)
v_mfma_f32_32x32x16_bf16 v[48:63], a[0:3], v[64:67], 0
ds_read_b128 v[76:79], v27 offset:30720
buffer_load_dwordx4 v[160:163], v2, s[12:15], s65 offen offset:2048 sc1
v_mul_f32 v112, 0xbacccccd, v120
v_mul_f32 v121, 0x3acccccd, v121
v_fma_f32 v121, -v112, v112, v121
v_max_f32 v121, 0, v121
v_add_f32 v121, 0x3727c5ac, v121
v_mfma_f32_32x32x16_bf16 v[48:63], a[40:43], v[68:71], v[48:63]
ds_read_b128 v[64:67], v27 offset:1024
v_rsq_f32 v116, v121
v_mul_f32 v114, 0xbacccccd, v122
v_mul_f32 v123, 0x3acccccd, v123
v_fma_f32 v123, -v114, v114, v123
v_max_f32 v123, 0, v123
s_waitcnt lgkmcnt(1)
v_mfma_f32_32x32x16_bf16 v[48:63], a[80:83], v[72:75], v[48:63]
ds_read_b128 v[68:71], v27 offset:11264
buffer_load_dwordx4 v[164:167], v2, s[12:15], s65 offen offset:3072 sc1
v_add_f32 v123, 0x3727c5ac, v123
v_rsq_f32 v118, v123
v_fmac_f32 v32, v96, v112
v_fmac_f32 v33, v97, v112
v_fma_f32 v32, v32, v116, v80
v_mfma_f32_32x32x16_bf16 v[48:63], a[120:123], v[76:79], v[48:63]
ds_read_b128 v[72:75], v27 offset:21504
v_fma_f32 v33, v33, v116, v81
v_mul_f32 v120, v32, v32
v_mul_f32 v121, v33, v33
v_fmaak_f32 v120, v120, v30, 0xc0135761
v_fmaak_f32 v121, v121, v30, 0xc0135761
s_waitcnt lgkmcnt(1)
v_mfma_f32_32x32x16_bf16 v[48:63], a[4:7], v[64:67], v[48:63]
ds_read_b128 v[76:79], v27 offset:31744
s_add_u32 s65, s65, 4096
buffer_load_dwordx4 v[168:171], v2, s[12:15], s65 offen offset:0 sc1
v_mul_f32 v120, v120, v32
v_mul_f32 v121, v121, v33
v_exp_f32 v120, v120
v_exp_f32 v121, v121
v_add_f32 v120, 1.0, v120
v_mfma_f32_32x32x16_bf16 v[48:63], a[44:47], v[68:71], v[48:63]
ds_read_b128 v[64:67], v27 offset:2048
v_add_f32 v121, 1.0, v121
v_rcp_f32 v120, v120
v_rcp_f32 v121, v121
v_mul_f32 v32, v32, v120
v_mul_f32 v33, v33, v121
s_waitcnt lgkmcnt(1)
v_mfma_f32_32x32x16_bf16 v[48:63], a[84:87], v[72:75], v[48:63]
ds_read_b128 v[68:71], v27 offset:12288
v_cvt_pk_bf16_f32 v32, v32, v33
v_fmac_f32 v34, v98, v112
v_fmac_f32 v35, v99, v112
v_fma_f32 v34, v34, v116, v82
v_mfma_f32_32x32x16_bf16 v[48:63], a[124:127], v[76:79], v[48:63]
ds_read_b128 v[72:75], v27 offset:22528
v_fma_f32 v35, v35, v116, v83
v_mul_f32 v122, v34, v34
v_mul_f32 v123, v35, v35
v_fmaak_f32 v122, v122, v30, 0xc0135761
s_waitcnt lgkmcnt(1)
v_mfma_f32_32x32x16_bf16 v[48:63], a[8:11], v[64:67], v[48:63]
ds_read_b128 v[76:79], v27 offset:32768
v_fmaak_f32 v123, v123, v30, 0xc0135761
v_mul_f32 v122, v122, v34
v_mul_f32 v123, v123, v35
v_exp_f32 v122, v122
v_mfma_f32_32x32x16_bf16 v[48:63], a[48:51], v[68:71], v[48:63]
ds_read_b128 v[64:67], v27 offset:3072
v_exp_f32 v123, v123
v_add_f32 v122, 1.0, v122
v_add_f32 v123, 1.0, v123
v_rcp_f32 v122, v122
s_waitcnt lgkmcnt(1)
v_mfma_f32_32x32x16_bf16 v[48:63], a[88:91], v[72:75], v[48:63]
ds_read_b128 v[68:71], v27 offset:13312
v_rcp_f32 v123, v123
v_mul_f32 v34, v34, v122
v_mul_f32 v35, v35, v123
v_cvt_pk_bf16_f32 v33, v34, v35
v_mfma_f32_32x32x16_bf16 v[48:63], a[128:131], v[76:79], v[48:63]
ds_read_b128 v[72:75], v27 offset:23552
v_fmac_f32 v36, v100, v112
v_fmac_f32 v37, v101, v112
v_fma_f32 v36, v36, v116, v84
v_fma_f32 v37, v37, v116, v85
s_waitcnt lgkmcnt(1)
v_mfma_f32_32x32x16_bf16 v[48:63], a[12:15], v[64:67], v[48:63]
ds_read_b128 v[76:79], v27 offset:33792
v_mul_f32 v120, v36, v36
v_mul_f32 v121, v37, v37
v_fmaak_f32 v120, v120, v30, 0xc0135761
v_fmaak_f32 v121, v121, v30, 0xc0135761
v_mfma_f32_32x32x16_bf16 v[48:63], a[52:55], v[68:71], v[48:63]
ds_read_b128 v[64:67], v27 offset:4096
v_mul_f32 v120, v120, v36
v_mul_f32 v121, v121, v37
v_exp_f32 v120, v120
v_exp_f32 v121, v121
s_waitcnt lgkmcnt(1)
v_mfma_f32_32x32x16_bf16 v[48:63], a[92:95], v[72:75], v[48:63]
ds_read_b128 v[68:71], v27 offset:14336
v_add_f32 v120, 1.0, v120
v_add_f32 v121, 1.0, v121
v_rcp_f32 v120, v120
v_rcp_f32 v121, v121
v_mfma_f32_32x32x16_bf16 v[48:63], a[132:135], v[76:79], v[48:63]
ds_read_b128 v[72:75], v27 offset:24576
v_mul_f32 v36, v36, v120
v_mul_f32 v37, v37, v121
v_cvt_pk_bf16_f32 v34, v36, v37
v_fmac_f32 v38, v102, v112
s_waitcnt lgkmcnt(1)
v_mfma_f32_32x32x16_bf16 v[48:63], a[16:19], v[64:67], v[48:63]
ds_read_b128 v[76:79], v27 offset:34816
v_fmac_f32 v39, v103, v112
v_fma_f32 v38, v38, v116, v86
v_fma_f32 v39, v39, v116, v87
v_mul_f32 v122, v38, v38
v_mfma_f32_32x32x16_bf16 v[48:63], a[56:59], v[68:71], v[48:63]
s_waitcnt vmcnt(11)
s_barrier
ds_read_b128 v[64:67], v27 offset:5120
v_mul_f32 v123, v39, v39
v_fmaak_f32 v122, v122, v30, 0xc0135761
v_fmaak_f32 v123, v123, v30, 0xc0135761
v_mul_f32 v122, v122, v38
s_waitcnt lgkmcnt(1)
v_mfma_f32_32x32x16_bf16 v[48:63], a[96:99], v[72:75], v[48:63]
ds_read_b128 v[68:71], v27 offset:15360
v_mul_f32 v123, v123, v39
v_exp_f32 v122, v122
v_exp_f32 v123, v123
v_add_f32 v122, 1.0, v122
v_mfma_f32_32x32x16_bf16 v[48:63], a[136:139], v[76:79], v[48:63]
ds_read_b128 v[72:75], v27 offset:25600
v_add_f32 v123, 1.0, v123
v_rcp_f32 v122, v122
v_rcp_f32 v123, v123
v_mul_f32 v38, v38, v122
s_waitcnt lgkmcnt(1)
v_mfma_f32_32x32x16_bf16 v[48:63], a[20:23], v[64:67], v[48:63]
ds_read_b128 v[76:79], v27 offset:35840
v_mul_f32 v39, v39, v123
v_cvt_pk_bf16_f32 v35, v38, v39
buffer_store_dwordx4 v[32:35], v28, s[12:15], s75 offen offset:0 sc1
v_fmac_f32 v40, v104, v112
v_mfma_f32_32x32x16_bf16 v[48:63], a[60:63], v[68:71], v[48:63]
ds_read_b128 v[64:67], v27 offset:6144
v_fmac_f32 v41, v105, v112
v_fma_f32 v40, v40, v116, v88
v_fma_f32 v41, v41, v116, v89
v_mul_f32 v120, v40, v40
s_waitcnt lgkmcnt(1)
v_mfma_f32_32x32x16_bf16 v[48:63], a[100:103], v[72:75], v[48:63]
ds_read_b128 v[68:71], v27 offset:16384
v_mul_f32 v121, v41, v41
v_fmaak_f32 v120, v120, v30, 0xc0135761
v_fmaak_f32 v121, v121, v30, 0xc0135761
v_mul_f32 v120, v120, v40
v_mfma_f32_32x32x16_bf16 v[48:63], a[140:143], v[76:79], v[48:63]
ds_read_b128 v[72:75], v27 offset:26624
v_mul_f32 v121, v121, v41
v_exp_f32 v120, v120
v_exp_f32 v121, v121
v_add_f32 v120, 1.0, v120
s_waitcnt lgkmcnt(1)
v_mfma_f32_32x32x16_bf16 v[48:63], a[24:27], v[64:67], v[48:63]
ds_read_b128 v[76:79], v27 offset:36864
v_add_f32 v121, 1.0, v121
v_rcp_f32 v120, v120
v_rcp_f32 v121, v121
v_mul_f32 v40, v40, v120
v_mfma_f32_32x32x16_bf16 v[48:63], a[64:67], v[68:71], v[48:63]
ds_read_b128 v[64:67], v27 offset:7168
v_mul_f32 v41, v41, v121
v_cvt_pk_bf16_f32 v40, v40, v41
v_fmac_f32 v42, v106, v112
v_fmac_f32 v43, v107, v112
s_waitcnt lgkmcnt(1)
v_mfma_f32_32x32x16_bf16 v[48:63], a[104:107], v[72:75], v[48:63]
ds_read_b128 v[68:71], v27 offset:17408
v_fma_f32 v42, v42, v116, v90
v_fma_f32 v43, v43, v116, v91
v_mul_f32 v122, v42, v42
v_mul_f32 v123, v43, v43
v_mfma_f32_32x32x16_bf16 v[48:63], a[144:147], v[76:79], v[48:63]
ds_read_b128 v[72:75], v27 offset:27648
v_fmaak_f32 v122, v122, v30, 0xc0135761
v_fmaak_f32 v123, v123, v30, 0xc0135761
v_mul_f32 v122, v122, v42
v_mul_f32 v123, v123, v43
s_waitcnt lgkmcnt(1)
v_mfma_f32_32x32x16_bf16 v[48:63], a[28:31], v[64:67], v[48:63]
ds_read_b128 v[76:79], v27 offset:37888
v_exp_f32 v122, v122
v_exp_f32 v123, v123
v_add_f32 v122, 1.0, v122
v_add_f32 v123, 1.0, v123
v_mfma_f32_32x32x16_bf16 v[48:63], a[68:71], v[68:71], v[48:63]
ds_read_b128 v[64:67], v27 offset:8192
v_rcp_f32 v122, v122
v_rcp_f32 v123, v123
v_mul_f32 v42, v42, v122
v_mul_f32 v43, v43, v123
s_waitcnt lgkmcnt(1)
v_mfma_f32_32x32x16_bf16 v[48:63], a[108:111], v[72:75], v[48:63]
ds_read_b128 v[68:71], v27 offset:18432
v_cvt_pk_bf16_f32 v41, v42, v43
v_fmac_f32 v44, v108, v112
v_fmac_f32 v45, v109, v112
v_fma_f32 v44, v44, v116, v92
v_mfma_f32_32x32x16_bf16 v[48:63], a[148:151], v[76:79], v[48:63]
ds_read_b128 v[72:75], v27 offset:28672
v_fma_f32 v45, v45, v116, v93
v_mul_f32 v120, v44, v44
v_mul_f32 v121, v45, v45
v_fmaak_f32 v120, v120, v30, 0xc0135761
s_waitcnt lgkmcnt(1)
v_mfma_f32_32x32x16_bf16 v[48:63], a[32:35], v[64:67], v[48:63]
ds_read_b128 v[76:79], v27 offset:38912
v_fmaak_f32 v121, v121, v30, 0xc0135761
v_mul_f32 v120, v120, v44
v_mul_f32 v121, v121, v45
v_exp_f32 v120, v120
v_mfma_f32_32x32x16_bf16 v[48:63], a[72:75], v[68:71], v[48:63]
ds_read_b128 v[64:67], v27 offset:9216
v_exp_f32 v121, v121
v_add_f32 v120, 1.0, v120
v_add_f32 v121, 1.0, v121
v_rcp_f32 v120, v120
s_waitcnt lgkmcnt(1)
v_mfma_f32_32x32x16_bf16 v[48:63], a[112:115], v[72:75], v[48:63]
ds_read_b128 v[68:71], v27 offset:19456
v_rcp_f32 v121, v121
v_mul_f32 v44, v44, v120
v_mul_f32 v45, v45, v121
v_cvt_pk_bf16_f32 v42, v44, v45
v_mfma_f32_32x32x16_bf16 v[48:63], a[152:155], v[76:79], v[48:63]
ds_read_b128 v[72:75], v27 offset:29696
v_fmac_f32 v46, v110, v112
v_fmac_f32 v47, v111, v112
v_fma_f32 v46, v46, v116, v94
v_fma_f32 v47, v47, v116, v95
s_waitcnt lgkmcnt(1)
v_mfma_f32_32x32x16_bf16 v[48:63], a[36:39], v[64:67], v[48:63]
ds_read_b128 v[76:79], v27 offset:39936
v_mul_f32 v122, v46, v46
v_mul_f32 v123, v47, v47
v_fmaak_f32 v122, v122, v30, 0xc0135761
v_fmaak_f32 v123, v123, v30, 0xc0135761
v_mfma_f32_32x32x16_bf16 v[48:63], a[76:79], v[68:71], v[48:63]
v_mul_f32 v122, v122, v46
v_mul_f32 v123, v123, v47
v_exp_f32 v122, v122
v_exp_f32 v123, v123
s_waitcnt lgkmcnt(0)
v_mfma_f32_32x32x16_bf16 v[48:63], a[116:119], v[72:75], v[48:63]
v_add_f32 v122, 1.0, v122
v_add_f32 v123, 1.0, v123
v_rcp_f32 v122, v122
v_rcp_f32 v123, v123
v_mfma_f32_32x32x16_bf16 v[48:63], a[156:159], v[76:79], v[48:63]
v_mul_f32 v46, v46, v122
v_mul_f32 v47, v47, v123
v_cvt_pk_bf16_f32 v43, v46, v47
buffer_store_dwordx4 v[40:43], v28, s[12:15], s75 offen offset:512 sc1
v_mov_b32 v124, v30
v_mov_b32 v125, v30
v_mov_b32 v126, 0xc0135761
v_mov_b32 v127, 0xc0135761
s_waitcnt vmcnt(0)
v_mov_b32 v18, s44
v_mov_b32 v19, s45
s_mov_b64 exec, 0xffff
buffer_store_dwordx2 v[18:19], v3, s[12:15], s77 offen sc1
s_mov_b64 exec, -1
s_add_u32 s77, s77, 6912
v_pk_fma_f32 v[48:49], v[96:97], v[114:115], v[48:49] op_sel_hi:[1,0,1]
v_pk_fma_f32 v[48:49], v[48:49], v[118:119], v[80:81] op_sel_hi:[1,0,1]
v_pk_mul_f32 v[120:121], v[48:49], v[48:49]
v_pk_fma_f32 v[120:121], v[120:121], v[124:125], v[126:127]
v_pk_mul_f32 v[120:121], v[120:121], v[48:49]
v_exp_f32 v120, v120
v_exp_f32 v121, v121
s_nop 0
v_pk_add_f32 v[120:121], v[120:121], 1.0 op_sel_hi:[1,0]
v_rcp_f32 v120, v120
v_rcp_f32 v121, v121
s_nop 0
v_pk_mul_f32 v[48:49], v[48:49], v[120:121]
v_cvt_pk_bf16_f32 v48, v48, v49
v_pk_fma_f32 v[50:51], v[98:99], v[114:115], v[50:51] op_sel_hi:[1,0,1]
v_pk_fma_f32 v[50:51], v[50:51], v[118:119], v[82:83] op_sel_hi:[1,0,1]
v_pk_mul_f32 v[122:123], v[50:51], v[50:51]
v_pk_fma_f32 v[122:123], v[122:123], v[124:125], v[126:127]
v_pk_mul_f32 v[122:123], v[122:123], v[50:51]
v_exp_f32 v122, v122
v_exp_f32 v123, v123
s_nop 0
v_pk_add_f32 v[122:123], v[122:123], 1.0 op_sel_hi:[1,0]
v_rcp_f32 v122, v122
v_rcp_f32 v123, v123
s_nop 0
v_pk_mul_f32 v[50:51], v[50:51], v[122:123]
v_cvt_pk_bf16_f32 v49, v50, v51
v_pk_fma_f32 v[52:53], v[100:101], v[114:115], v[52:53] op_sel_hi:[1,0,1]
v_pk_fma_f32 v[52:53], v[52:53], v[118:119], v[84:85] op_sel_hi:[1,0,1]
v_pk_mul_f32 v[120:121], v[52:53], v[52:53]
)ASM"
      R"ASM(
v_pk_fma_f32 v[120:121], v[120:121], v[124:125], v[126:127]
v_pk_mul_f32 v[120:121], v[120:121], v[52:53]
v_exp_f32 v120, v120
v_exp_f32 v121, v121
s_nop 0
v_pk_add_f32 v[120:121], v[120:121], 1.0 op_sel_hi:[1,0]
v_rcp_f32 v120, v120
v_rcp_f32 v121, v121
s_nop 0
v_pk_mul_f32 v[52:53], v[52:53], v[120:121]
v_cvt_pk_bf16_f32 v50, v52, v53
v_pk_fma_f32 v[54:55], v[102:103], v[114:115], v[54:55] op_sel_hi:[1,0,1]
v_pk_fma_f32 v[54:55], v[54:55], v[118:119], v[86:87] op_sel_hi:[1,0,1]
v_pk_mul_f32 v[122:123], v[54:55], v[54:55]
v_pk_fma_f32 v[122:123], v[122:123], v[124:125], v[126:127]
v_pk_mul_f32 v[122:123], v[122:123], v[54:55]
v_exp_f32 v122, v122
v_exp_f32 v123, v123
s_nop 0
v_pk_add_f32 v[122:123], v[122:123], 1.0 op_sel_hi:[1,0]
v_rcp_f32 v122, v122
v_rcp_f32 v123, v123
s_nop 0
v_pk_mul_f32 v[54:55], v[54:55], v[122:123]
v_cvt_pk_bf16_f32 v51, v54, v55
buffer_store_dwordx4 v[48:51], v28, s[12:15], s76 offen offset:0 sc1
v_pk_fma_f32 v[56:57], v[104:105], v[114:115], v[56:57] op_sel_hi:[1,0,1]
v_pk_fma_f32 v[56:57], v[56:57], v[118:119], v[88:89] op_sel_hi:[1,0,1]
v_pk_mul_f32 v[120:121], v[56:57], v[56:57]
v_pk_fma_f32 v[120:121], v[120:121], v[124:125], v[126:127]
v_pk_mul_f32 v[120:121], v[120:121], v[56:57]
v_exp_f32 v120, v120
v_exp_f32 v121, v121
s_nop 0
v_pk_add_f32 v[120:121], v[120:121], 1.0 op_sel_hi:[1,0]
v_rcp_f32 v120, v120
v_rcp_f32 v121, v121
s_nop 0
v_pk_mul_f32 v[56:57], v[56:57], v[120:121]
v_cvt_pk_bf16_f32 v56, v56, v57
v_pk_fma_f32 v[58:59], v[106:107], v[114:115], v[58:59] op_sel_hi:[1,0,1]
v_pk_fma_f32 v[58:59], v[58:59], v[118:119], v[90:91] op_sel_hi:[1,0,1]
v_pk_mul_f32 v[122:123], v[58:59], v[58:59]
v_pk_fma_f32 v[122:123], v[122:123], v[124:125], v[126:127]
v_pk_mul_f32 v[122:123], v[122:123], v[58:59]
v_exp_f32 v122, v122
v_exp_f32 v123, v123
s_nop 0
v_pk_add_f32 v[122:123], v[122:123], 1.0 op_sel_hi:[1,0]
v_rcp_f32 v122, v122
v_rcp_f32 v123, v123
s_nop 0
v_pk_mul_f32 v[58:59], v[58:59], v[122:123]
v_cvt_pk_bf16_f32 v57, v58, v59
v_pk_fma_f32 v[60:61], v[108:109], v[114:115], v[60:61] op_sel_hi:[1,0,1]
v_pk_fma_f32 v[60:61], v[60:61], v[118:119], v[92:93] op_sel_hi:[1,0,1]
v_pk_mul_f32 v[120:121], v[60:61], v[60:61]
v_pk_fma_f32 v[120:121], v[120:121], v[124:125], v[126:127]
v_pk_mul_f32 v[120:121], v[120:121], v[60:61]
v_exp_f32 v120, v120
v_exp_f32 v121, v121
s_nop 0
v_pk_add_f32 v[120:121], v[120:121], 1.0 op_sel_hi:[1,0]
v_rcp_f32 v120, v120
v_rcp_f32 v121, v121
s_nop 0
v_pk_mul_f32 v[60:61], v[60:61], v[120:121]
v_cvt_pk_bf16_f32 v58, v60, v61
v_pk_fma_f32 v[62:63], v[110:111], v[114:115], v[62:63] op_sel_hi:[1,0,1]
v_pk_fma_f32 v[62:63], v[62:63], v[118:119], v[94:95] op_sel_hi:[1,0,1]
v_pk_mul_f32 v[122:123], v[62:63], v[62:63]
v_pk_fma_f32 v[122:123], v[122:123], v[124:125], v[126:127]
v_pk_mul_f32 v[122:123], v[122:123], v[62:63]
v_exp_f32 v122, v122
v_exp_f32 v123, v123
s_nop 0
v_pk_add_f32 v[122:123], v[122:123], 1.0 op_sel_hi:[1,0]
v_rcp_f32 v122, v122
v_rcp_f32 v123, v123
s_nop 0
v_pk_mul_f32 v[62:63], v[62:63], v[122:123]
v_cvt_pk_bf16_f32 v59, v62, v63
buffer_store_dwordx4 v[56:59], v28, s[12:15], s76 offen offset:512 sc1
s_waitcnt vmcnt(0)
v_mov_b32 v18, s44
v_mov_b32 v19, s45
s_mov_b64 exec, 0xffff
buffer_store_dwordx2 v[18:19], v3, s[12:15], s77 offen sc1
s_mov_b64 exec, -1
v_bfe_u32 v16, v0, 3, 1
v_and_b32 v17, 7, v0
v_lshl_add_u32 v16, v16, 5, v17
v_lshlrev_b32 v16, 7, v16
s_lshl_b32 s50, s72, 3
s_add_u32 s50, s50, s47
s_lshl_b32 s50, s50, 10
s_add_u32 s50, s50, 655360
s_add_u32 s51, s70, 4
s_mul_i32 s52, s51, 40960
s_mul_i32 s53, s47, 10240
s_add_u32 s52, s52, s53
s_add_u32 s73, s52, 13631488
s_add_u32 s74, s53, 81920
s_mul_i32 s52, s51, 110592
s_lshl_b32 s53, s92, 10
s_add_u32 s52, s52, s53
s_add_u32 s75, s52, 16777216
s_cmp_ge_u32 s92, 54
s_cselect_b32 s75, 0x40000000, s75
s_add_u32 s76, s75, 110592
s_lshl_b32 s52, s51, 1
s_add_u32 s52, s52, s47
s_lshl_b32 s52, s52, 11
s_add_u32 s78, s52, 16252928
s_mul_i32 s52, s72, 108
s_add_u32 s52, s52, s92
s_lshl_b32 s52, s52, 7
s_add_u32 s77, s52, 1007616
s_cmp_ge_u32 s92, 54
s_cselect_b32 s77, 0x40000000, s77
v_add_u32 v26, 81920, v2
v_add_u32 v27, 122880, v2
s_lshl_b32 s52, s47, 7
s_add_u32 s52, s52, 0
v_and_b32 v17, 15, v0
v_lshl_add_u32 v31, v17, 3, s52
v_lshrrev_b32 v17, 5, v0
v_lshl_add_u32 v31, v17, 2, v31
.Lmk_poll_44:
buffer_load_dwordx2 v[18:19], v16, s[12:15], s50 offen sc1
s_waitcnt vmcnt(0)
v_cmp_ne_u64 vcc, s[44:45], v[18:19]
s_nop 0
s_cbranch_vccz .Lmk_polld_45
s_sub_u32 s46, s46, 1
s_cbranch_scc0 .Lmk_poll_44
s_mov_b32 s46, 0
.Lmk_polld_45:
s_add_u32 m0, s74, 0
s_add_u32 s55, s73, 0
buffer_load_dwordx4 v2, s[12:15], s55 offen offset:0 sc1 lds
buffer_load_dwordx4 v2, s[12:15], s55 offen offset:1024 sc1 lds
buffer_load_dwordx4 v2, s[12:15], s55 offen offset:2048 sc1 lds
buffer_load_dwordx4 v2, s[12:15], s55 offen offset:3072 sc1 lds
s_add_u32 m0, s74, 4096
s_add_u32 s55, s73, 4096
buffer_load_dwordx4 v2, s[12:15], s55 offen offset:0 sc1 lds
buffer_load_dwordx4 v2, s[12:15], s55 offen offset:1024 sc1 lds
buffer_load_dwordx4 v2, s[12:15], s55 offen offset:2048 sc1 lds
buffer_load_dwordx4 v2, s[12:15], s55 offen offset:3072 sc1 lds
s_add_u32 m0, s74, 8192
s_add_u32 s55, s73, 8192
buffer_load_dwordx4 v2, s[12:15], s55 offen offset:0 sc1 lds
buffer_load_dwordx4 v2, s[12:15], s55 offen offset:1024 sc1 lds
s_waitcnt vmcnt(8)
s_barrier
ds_read_b128 v[64:67], v26 offset:0
ds_read_b128 v[68:71], v26 offset:10240
ds_read_b128 v[72:75], v26 offset:20480
s_waitcnt lgkmcnt(1)
v_mfma_f32_32x32x16_bf16 v[32:47], a[0:3], v[64:67], 0
ds_read_b128 v[76:79], v26 offset:30720
buffer_load_dwordx2 v[120:121], v29, s[12:15], s78 offen offset:0 sc1
v_mfma_f32_32x32x16_bf16 v[32:47], a[40:43], v[68:71], v[32:47]
ds_read_b128 v[64:67], v26 offset:1024
buffer_load_dwordx2 v[122:123], v29, s[12:15], s78 offen offset:128 sc1
s_waitcnt lgkmcnt(1)
v_mfma_f32_32x32x16_bf16 v[32:47], a[80:83], v[72:75], v[32:47]
ds_read_b128 v[68:71], v26 offset:11264
buffer_load_dwordx2 v[124:125], v29, s[12:15], s78 offen offset:256 sc1
v_mfma_f32_32x32x16_bf16 v[32:47], a[120:123], v[76:79], v[32:47]
ds_read_b128 v[72:75], v26 offset:21504
buffer_load_dwordx2 v[126:127], v29, s[12:15], s78 offen offset:384 sc1
s_waitcnt lgkmcnt(1)
v_mfma_f32_32x32x16_bf16 v[32:47], a[4:7], v[64:67], v[32:47]
ds_read_b128 v[76:79], v26 offset:31744
s_add_u32 m0, s74, 40960
s_add_u32 s55, s73, 40960
buffer_load_dwordx4 v2, s[12:15], s55 offen offset:0 sc1 lds
v_mfma_f32_32x32x16_bf16 v[32:47], a[44:47], v[68:71], v[32:47]
s_waitcnt vmcnt(9)
s_barrier
ds_read_b128 v[64:67], v26 offset:2048
s_waitcnt lgkmcnt(1)
v_mfma_f32_32x32x16_bf16 v[32:47], a[84:87], v[72:75], v[32:47]
ds_read_b128 v[68:71], v26 offset:12288
buffer_load_dwordx4 v2, s[12:15], s55 offen offset:1024 sc1 lds
v_mfma_f32_32x32x16_bf16 v[32:47], a[124:127], v[76:79], v[32:47]
ds_read_b128 v[72:75], v26 offset:22528
s_waitcnt lgkmcnt(1)
v_mfma_f32_32x32x16_bf16 v[32:47], a[8:11], v[64:67], v[32:47]
ds_read_b128 v[76:79], v26 offset:32768
buffer_load_dwordx4 v2, s[12:15], s55 offen offset:2048 sc1 lds
v_mfma_f32_32x32x16_bf16 v[32:47], a[48:51], v[68:71], v[32:47]
ds_read_b128 v[64:67], v26 offset:3072
s_waitcnt lgkmcnt(1)
v_mfma_f32_32x32x16_bf16 v[32:47], a[88:91], v[72:75], v[32:47]
ds_read_b128 v[68:71], v26 offset:13312
buffer_load_dwordx4 v2, s[12:15], s55 offen offset:3072 sc1 lds
v_mfma_f32_32x32x16_bf16 v[32:47], a[128:131], v[76:79], v[32:47]
ds_read_b128 v[72:75], v26 offset:23552
s_waitcnt lgkmcnt(1)
v_mfma_f32_32x32x16_bf16 v[32:47], a[12:15], v[64:67], v[32:47]
ds_read_b128 v[76:79], v26 offset:33792
s_add_u32 m0, s74, 45056
s_add_u32 s55, s73, 45056
buffer_load_dwordx4 v2, s[12:15], s55 offen offset:0 sc1 lds
v_mfma_f32_32x32x16_bf16 v[32:47], a[52:55], v[68:71], v[32:47]
ds_read_b128 v[64:67], v26 offset:4096
s_waitcnt lgkmcnt(1)
v_mfma_f32_32x32x16_bf16 v[32:47], a[92:95], v[72:75], v[32:47]
ds_read_b128 v[68:71], v26 offset:14336
buffer_load_dwordx4 v2, s[12:15], s55 offen offset:1024 sc1 lds
v_mfma_f32_32x32x16_bf16 v[32:47], a[132:135], v[76:79], v[32:47]
ds_read_b128 v[72:75], v26 offset:24576
s_waitcnt lgkmcnt(1)
v_mfma_f32_32x32x16_bf16 v[32:47], a[16:19], v[64:67], v[32:47]
ds_read_b128 v[76:79], v26 offset:34816
buffer_load_dwordx4 v2, s[12:15], s55 offen offset:2048 sc1 lds
v_mfma_f32_32x32x16_bf16 v[32:47], a[56:59], v[68:71], v[32:47]
ds_read_b128 v[64:67], v26 offset:5120
s_waitcnt lgkmcnt(1)
v_mfma_f32_32x32x16_bf16 v[32:47], a[96:99], v[72:75], v[32:47]
ds_read_b128 v[68:71], v26 offset:15360
buffer_load_dwordx4 v2, s[12:15], s55 offen offset:3072 sc1 lds
v_mfma_f32_32x32x16_bf16 v[32:47], a[136:139], v[76:79], v[32:47]
ds_read_b128 v[72:75], v26 offset:25600
s_waitcnt lgkmcnt(1)
v_mfma_f32_32x32x16_bf16 v[32:47], a[20:23], v[64:67], v[32:47]
ds_read_b128 v[76:79], v26 offset:35840
s_add_u32 m0, s74, 49152
s_add_u32 s55, s73, 49152
buffer_load_dwordx4 v2, s[12:15], s55 offen offset:0 sc1 lds
v_mfma_f32_32x32x16_bf16 v[32:47], a[60:63], v[68:71], v[32:47]
s_waitcnt vmcnt(13)
s_barrier
ds_read_b128 v[64:67], v26 offset:6144
s_waitcnt lgkmcnt(1)
v_mfma_f32_32x32x16_bf16 v[32:47], a[100:103], v[72:75], v[32:47]
ds_read_b128 v[68:71], v26 offset:16384
buffer_load_dwordx4 v2, s[12:15], s55 offen offset:1024 sc1 lds
v_mfma_f32_32x32x16_bf16 v[32:47], a[140:143], v[76:79], v[32:47]
ds_read_b128 v[72:75], v26 offset:26624
s_waitcnt lgkmcnt(1)
v_mfma_f32_32x32x16_bf16 v[32:47], a[24:27], v[64:67], v[32:47]
ds_read_b128 v[76:79], v26 offset:36864
buffer_load_dwordx4 v[172:175], v2, s[12:15], s65 offen offset:1024 sc1
v_mfma_f32_32x32x16_bf16 v[32:47], a[64:67], v[68:71], v[32:47]
ds_read_b128 v[64:67], v26 offset:7168
s_waitcnt lgkmcnt(1)
v_mfma_f32_32x32x16_bf16 v[32:47], a[104:107], v[72:75], v[32:47]
ds_read_b128 v[68:71], v26 offset:17408
buffer_load_dwordx4 v[176:179], v2, s[12:15], s65 offen offset:2048 sc1
s_waitcnt vmcnt(12)
v_add_f32 v120, v120, v124
v_add_f32 v121, v121, v125
v_add_f32 v122, v122, v126
v_add_f32 v123, v123, v127
v_mfma_f32_32x32x16_bf16 v[32:47], a[144:147], v[76:79], v[32:47]
ds_read_b128 v[72:75], v26 offset:27648
v_add_f32 v120, v120, v122
v_add_f32 v121, v121, v123
s_waitcnt lgkmcnt(1)
v_mfma_f32_32x32x16_bf16 v[32:47], a[28:31], v[64:67], v[32:47]
ds_read_b128 v[76:79], v26 offset:37888
buffer_load_dwordx4 v[180:183], v2, s[12:15], s65 offen offset:3072 sc1
v_mfma_f32_32x32x16_bf16 v[32:47], a[68:71], v[68:71], v[32:47]
ds_read_b128 v[64:67], v26 offset:8192
s_nop 2
v_permlane32_swap_b32 v120, v121
v_add_f32 v120, v120, v121
v_mov_b32 v121, v120
s_nop 1
v_permlane16_swap_b32 v120, v121
v_add_f32 v120, v120, v121
s_waitcnt lgkmcnt(1)
v_mfma_f32_32x32x16_bf16 v[32:47], a[108:111], v[72:75], v[32:47]
ds_read_b128 v[68:71], v26 offset:18432
s_add_u32 s65, s65, 4096
buffer_load_dwordx4 v[184:187], v2, s[12:15], s65 offen offset:0 sc1
v_mfma_f32_32x32x16_bf16 v[32:47], a[148:151], v[76:79], v[32:47]
ds_read_b128 v[72:75], v26 offset:28672
ds_write_b32 v31, v120
s_waitcnt lgkmcnt(2)
v_mfma_f32_32x32x16_bf16 v[32:47], a[32:35], v[64:67], v[32:47]
ds_read_b128 v[76:79], v26 offset:38912
buffer_load_dwordx4 v[188:191], v2, s[12:15], s65 offen offset:1024 sc1
v_mfma_f32_32x32x16_bf16 v[32:47], a[72:75], v[68:71], v[32:47]
ds_read_b128 v[64:67], v26 offset:9216
s_waitcnt lgkmcnt(2)
s_waitcnt lgkmcnt(1)
v_mfma_f32_32x32x16_bf16 v[32:47], a[112:115], v[72:75], v[32:47]
ds_read_b128 v[68:71], v26 offset:19456
buffer_load_dwordx4 v[192:195], v2, s[12:15], s65 offen offset:2048 sc1
v_mfma_f32_32x32x16_bf16 v[32:47], a[152:155], v[76:79], v[32:47]
ds_read_b128 v[72:75], v26 offset:29696
s_waitcnt lgkmcnt(1)
v_mfma_f32_32x32x16_bf16 v[32:47], a[36:39], v[64:67], v[32:47]
ds_read_b128 v[76:79], v26 offset:39936
buffer_load_dwordx4 v[196:199], v2, s[12:15], s65 offen offset:3072 sc1
v_mfma_f32_32x32x16_bf16 v[32:47], a[76:79], v[68:71], v[32:47]
s_waitcnt vmcnt(12)
s_barrier
ds_read_b128 v[64:67], v27 offset:0
v_and_b32 v16, 31, v0
v_lshlrev_b32 v16, 3, v16
ds_read_b64 v[120:121], v16 offset:0
ds_read_b64 v[122:123], v16 offset:256
s_waitcnt lgkmcnt(3)
v_mfma_f32_32x32x16_bf16 v[32:47], a[116:119], v[72:75], v[32:47]
ds_read_b128 v[68:71], v27 offset:10240
s_add_u32 s65, s65, 4096
buffer_load_dwordx4 v[200:203], v2, s[12:15], s65 offen offset:0 sc1
v_mfma_f32_32x32x16_bf16 v[32:47], a[156:159], v[76:79], v[32:47]
ds_read_b128 v[72:75], v27 offset:20480
s_waitcnt lgkmcnt(1)
v_mfma_f32_32x32x16_bf16 v[48:63], a[0:3], v[64:67], 0
ds_read_b128 v[76:79], v27 offset:30720
buffer_load_dwordx4 v[204:207], v2, s[12:15], s65 offen offset:1024 sc1
v_mul_f32 v112, 0xbacccccd, v120
v_mul_f32 v121, 0x3acccccd, v121
v_fma_f32 v121, -v112, v112, v121
v_max_f32 v121, 0, v121
v_add_f32 v121, 0x3727c5ac, v121
v_mfma_f32_32x32x16_bf16 v[48:63], a[40:43], v[68:71], v[48:63]
ds_read_b128 v[64:67], v27 offset:1024
v_rsq_f32 v116, v121
v_mul_f32 v114, 0xbacccccd, v122
v_mul_f32 v123, 0x3acccccd, v123
v_fma_f32 v123, -v114, v114, v123
v_max_f32 v123, 0, v123
s_waitcnt lgkmcnt(1)
v_mfma_f32_32x32x16_bf16 v[48:63], a[80:83], v[72:75], v[48:63]
ds_read_b128 v[68:71], v27 offset:11264
buffer_load_dwordx4 v[208:211], v2, s[12:15], s65 offen offset:2048 sc1
v_add_f32 v123, 0x3727c5ac, v123
v_rsq_f32 v118, v123
v_fmac_f32 v32, v96, v112
v_fmac_f32 v33, v97, v112
v_fma_f32 v32, v32, v116, v80
v_mfma_f32_32x32x16_bf16 v[48:63], a[120:123], v[76:79], v[48:63]
ds_read_b128 v[72:75], v27 offset:21504
v_fma_f32 v33, v33, v116, v81
v_mul_f32 v120, v32, v32
v_mul_f32 v121, v33, v33
v_fmaak_f32 v120, v120, v30, 0xc0135761
v_fmaak_f32 v121, v121, v30, 0xc0135761
s_waitcnt lgkmcnt(1)
v_mfma_f32_32x32x16_bf16 v[48:63], a[4:7], v[64:67], v[48:63]
ds_read_b128 v[76:79], v27 offset:31744
buffer_load_dwordx4 v[212:215], v2, s[12:15], s65 offen offset:3072 sc1
v_mul_f32 v120, v120, v32
v_mul_f32 v121, v121, v33
v_exp_f32 v120, v120
v_exp_f32 v121, v121
v_add_f32 v120, 1.0, v120
v_mfma_f32_32x32x16_bf16 v[48:63], a[44:47], v[68:71], v[48:63]
ds_read_b128 v[64:67], v27 offset:2048
v_add_f32 v121, 1.0, v121
v_rcp_f32 v120, v120
v_rcp_f32 v121, v121
v_mul_f32 v32, v32, v120
v_mul_f32 v33, v33, v121
s_waitcnt lgkmcnt(1)
v_mfma_f32_32x32x16_bf16 v[48:63], a[84:87], v[72:75], v[48:63]
ds_read_b128 v[68:71], v27 offset:12288
v_cvt_pk_bf16_f32 v32, v32, v33
v_fmac_f32 v34, v98, v112
v_fmac_f32 v35, v99, v112
v_fma_f32 v34, v34, v116, v82
v_mfma_f32_32x32x16_bf16 v[48:63], a[124:127], v[76:79], v[48:63]
ds_read_b128 v[72:75], v27 offset:22528
v_fma_f32 v35, v35, v116, v83
v_mul_f32 v122, v34, v34
v_mul_f32 v123, v35, v35
v_fmaak_f32 v122, v122, v30, 0xc0135761
s_waitcnt lgkmcnt(1)
v_mfma_f32_32x32x16_bf16 v[48:63], a[8:11], v[64:67], v[48:63]
ds_read_b128 v[76:79], v27 offset:32768
v_fmaak_f32 v123, v123, v30, 0xc0135761
v_mul_f32 v122, v122, v34
v_mul_f32 v123, v123, v35
v_exp_f32 v122, v122
v_mfma_f32_32x32x16_bf16 v[48:63], a[48:51], v[68:71], v[48:63]
ds_read_b128 v[64:67], v27 offset:3072
v_exp_f32 v123, v123
v_add_f32 v122, 1.0, v122
v_add_f32 v123, 1.0, v123
v_rcp_f32 v122, v122
s_waitcnt lgkmcnt(1)
v_mfma_f32_32x32x16_bf16 v[48:63], a[88:91], v[72:75], v[48:63]
ds_read_b128 v[68:71], v27 offset:13312
v_rcp_f32 v123, v123
v_mul_f32 v34, v34, v122
v_mul_f32 v35, v35, v123
v_cvt_pk_bf16_f32 v33, v34, v35
v_mfma_f32_32x32x16_bf16 v[48:63], a[128:131], v[76:79], v[48:63]
ds_read_b128 v[72:75], v27 offset:23552
v_fmac_f32 v36, v100, v112
v_fmac_f32 v37, v101, v112
)ASM"
      R"ASM(
v_fma_f32 v36, v36, v116, v84
v_fma_f32 v37, v37, v116, v85
s_waitcnt lgkmcnt(1)
v_mfma_f32_32x32x16_bf16 v[48:63], a[12:15], v[64:67], v[48:63]
ds_read_b128 v[76:79], v27 offset:33792
v_mul_f32 v120, v36, v36
v_mul_f32 v121, v37, v37
v_fmaak_f32 v120, v120, v30, 0xc0135761
v_fmaak_f32 v121, v121, v30, 0xc0135761
v_mfma_f32_32x32x16_bf16 v[48:63], a[52:55], v[68:71], v[48:63]
ds_read_b128 v[64:67], v27 offset:4096
v_mul_f32 v120, v120, v36
v_mul_f32 v121, v121, v37
v_exp_f32 v120, v120
v_exp_f32 v121, v121
s_waitcnt lgkmcnt(1)
v_mfma_f32_32x32x16_bf16 v[48:63], a[92:95], v[72:75], v[48:63]
ds_read_b128 v[68:71], v27 offset:14336
v_add_f32 v120, 1.0, v120
v_add_f32 v121, 1.0, v121
v_rcp_f32 v120, v120
v_rcp_f32 v121, v121
v_mfma_f32_32x32x16_bf16 v[48:63], a[132:135], v[76:79], v[48:63]
ds_read_b128 v[72:75], v27 offset:24576
v_mul_f32 v36, v36, v120
v_mul_f32 v37, v37, v121
v_cvt_pk_bf16_f32 v34, v36, v37
v_fmac_f32 v38, v102, v112
s_waitcnt lgkmcnt(1)
v_mfma_f32_32x32x16_bf16 v[48:63], a[16:19], v[64:67], v[48:63]
ds_read_b128 v[76:79], v27 offset:34816
v_fmac_f32 v39, v103, v112
v_fma_f32 v38, v38, v116, v86
v_fma_f32 v39, v39, v116, v87
v_mul_f32 v122, v38, v38
v_mfma_f32_32x32x16_bf16 v[48:63], a[56:59], v[68:71], v[48:63]
s_waitcnt vmcnt(11)
s_barrier
ds_read_b128 v[64:67], v27 offset:5120
v_mul_f32 v123, v39, v39
v_fmaak_f32 v122, v122, v30, 0xc0135761
v_fmaak_f32 v123, v123, v30, 0xc0135761
v_mul_f32 v122, v122, v38
s_waitcnt lgkmcnt(1)
v_mfma_f32_32x32x16_bf16 v[48:63], a[96:99], v[72:75], v[48:63]
ds_read_b128 v[68:71], v27 offset:15360
v_mul_f32 v123, v123, v39
v_exp_f32 v122, v122
v_exp_f32 v123, v123
v_add_f32 v122, 1.0, v122
v_mfma_f32_32x32x16_bf16 v[48:63], a[136:139], v[76:79], v[48:63]
ds_read_b128 v[72:75], v27 offset:25600
v_add_f32 v123, 1.0, v123
v_rcp_f32 v122, v122
v_rcp_f32 v123, v123
v_mul_f32 v38, v38, v122
s_waitcnt lgkmcnt(1)
v_mfma_f32_32x32x16_bf16 v[48:63], a[20:23], v[64:67], v[48:63]
ds_read_b128 v[76:79], v27 offset:35840
v_mul_f32 v39, v39, v123
v_cvt_pk_bf16_f32 v35, v38, v39
buffer_store_dwordx4 v[32:35], v28, s[12:15], s75 offen offset:0 sc1
v_fmac_f32 v40, v104, v112
v_mfma_f32_32x32x16_bf16 v[48:63], a[60:63], v[68:71], v[48:63]
ds_read_b128 v[64:67], v27 offset:6144
v_fmac_f32 v41, v105, v112
v_fma_f32 v40, v40, v116, v88
v_fma_f32 v41, v41, v116, v89
v_mul_f32 v120, v40, v40
s_waitcnt lgkmcnt(1)
v_mfma_f32_32x32x16_bf16 v[48:63], a[100:103], v[72:75], v[48:63]
ds_read_b128 v[68:71], v27 offset:16384
v_mul_f32 v121, v41, v41
v_fmaak_f32 v120, v120, v30, 0xc0135761
v_fmaak_f32 v121, v121, v30, 0xc0135761
v_mul_f32 v120, v120, v40
v_mfma_f32_32x32x16_bf16 v[48:63], a[140:143], v[76:79], v[48:63]
ds_read_b128 v[72:75], v27 offset:26624
v_mul_f32 v121, v121, v41
v_exp_f32 v120, v120
v_exp_f32 v121, v121
v_add_f32 v120, 1.0, v120
s_waitcnt lgkmcnt(1)
v_mfma_f32_32x32x16_bf16 v[48:63], a[24:27], v[64:67], v[48:63]
ds_read_b128 v[76:79], v27 offset:36864
v_add_f32 v121, 1.0, v121
v_rcp_f32 v120, v120
v_rcp_f32 v121, v121
v_mul_f32 v40, v40, v120
v_mfma_f32_32x32x16_bf16 v[48:63], a[64:67], v[68:71], v[48:63]
ds_read_b128 v[64:67], v27 offset:7168
v_mul_f32 v41, v41, v121
v_cvt_pk_bf16_f32 v40, v40, v41
v_fmac_f32 v42, v106, v112
v_fmac_f32 v43, v107, v112
s_waitcnt lgkmcnt(1)
v_mfma_f32_32x32x16_bf16 v[48:63], a[104:107], v[72:75], v[48:63]
ds_read_b128 v[68:71], v27 offset:17408
v_fma_f32 v42, v42, v116, v90
v_fma_f32 v43, v43, v116, v91
v_mul_f32 v122, v42, v42
v_mul_f32 v123, v43, v43
v_mfma_f32_32x32x16_bf16 v[48:63], a[144:147], v[76:79], v[48:63]
ds_read_b128 v[72:75], v27 offset:27648
v_fmaak_f32 v122, v122, v30, 0xc0135761
v_fmaak_f32 v123, v123, v30, 0xc0135761
v_mul_f32 v122, v122, v42
v_mul_f32 v123, v123, v43
s_waitcnt lgkmcnt(1)
v_mfma_f32_32x32x16_bf16 v[48:63], a[28:31], v[64:67], v[48:63]
ds_read_b128 v[76:79], v27 offset:37888
v_exp_f32 v122, v122
v_exp_f32 v123, v123
v_add_f32 v122, 1.0, v122
v_add_f32 v123, 1.0, v123
v_mfma_f32_32x32x16_bf16 v[48:63], a[68:71], v[68:71], v[48:63]
ds_read_b128 v[64:67], v27 offset:8192
v_rcp_f32 v122, v122
v_rcp_f32 v123, v123
v_mul_f32 v42, v42, v122
v_mul_f32 v43, v43, v123
s_waitcnt lgkmcnt(1)
v_mfma_f32_32x32x16_bf16 v[48:63], a[108:111], v[72:75], v[48:63]
ds_read_b128 v[68:71], v27 offset:18432
v_cvt_pk_bf16_f32 v41, v42, v43
v_fmac_f32 v44, v108, v112
v_fmac_f32 v45, v109, v112
v_fma_f32 v44, v44, v116, v92
v_mfma_f32_32x32x16_bf16 v[48:63], a[148:151], v[76:79], v[48:63]
ds_read_b128 v[72:75], v27 offset:28672
v_fma_f32 v45, v45, v116, v93
v_mul_f32 v120, v44, v44
v_mul_f32 v121, v45, v45
v_fmaak_f32 v120, v120, v30, 0xc0135761
s_waitcnt lgkmcnt(1)
v_mfma_f32_32x32x16_bf16 v[48:63], a[32:35], v[64:67], v[48:63]
ds_read_b128 v[76:79], v27 offset:38912
v_fmaak_f32 v121, v121, v30, 0xc0135761
v_mul_f32 v120, v120, v44
v_mul_f32 v121, v121, v45
v_exp_f32 v120, v120
v_mfma_f32_32x32x16_bf16 v[48:63], a[72:75], v[68:71], v[48:63]
ds_read_b128 v[64:67], v27 offset:9216
v_exp_f32 v121, v121
v_add_f32 v120, 1.0, v120
v_add_f32 v121, 1.0, v121
v_rcp_f32 v120, v120
s_waitcnt lgkmcnt(1)
v_mfma_f32_32x32x16_bf16 v[48:63], a[112:115], v[72:75], v[48:63]
ds_read_b128 v[68:71], v27 offset:19456
v_rcp_f32 v121, v121
v_mul_f32 v44, v44, v120
v_mul_f32 v45, v45, v121
v_cvt_pk_bf16_f32 v42, v44, v45
v_mfma_f32_32x32x16_bf16 v[48:63], a[152:155], v[76:79], v[48:63]
ds_read_b128 v[72:75], v27 offset:29696
v_fmac_f32 v46, v110, v112
v_fmac_f32 v47, v111, v112
v_fma_f32 v46, v46, v116, v94
v_fma_f32 v47, v47, v116, v95
s_waitcnt lgkmcnt(1)
v_mfma_f32_32x32x16_bf16 v[48:63], a[36:39], v[64:67], v[48:63]
ds_read_b128 v[76:79], v27 offset:39936
v_mul_f32 v122, v46, v46
v_mul_f32 v123, v47, v47
v_fmaak_f32 v122, v122, v30, 0xc0135761
v_fmaak_f32 v123, v123, v30, 0xc0135761
v_mfma_f32_32x32x16_bf16 v[48:63], a[76:79], v[68:71], v[48:63]
v_mul_f32 v122, v122, v46
v_mul_f32 v123, v123, v47
v_exp_f32 v122, v122
v_exp_f32 v123, v123
s_waitcnt lgkmcnt(0)
v_mfma_f32_32x32x16_bf16 v[48:63], a[116:119], v[72:75], v[48:63]
v_add_f32 v122, 1.0, v122
v_add_f32 v123, 1.0, v123
v_rcp_f32 v122, v122
v_rcp_f32 v123, v123
v_mfma_f32_32x32x16_bf16 v[48:63], a[156:159], v[76:79], v[48:63]
v_mul_f32 v46, v46, v122
v_mul_f32 v47, v47, v123
v_cvt_pk_bf16_f32 v43, v46, v47
buffer_store_dwordx4 v[40:43], v28, s[12:15], s75 offen offset:512 sc1
v_mov_b32 v124, v30
v_mov_b32 v125, v30
v_mov_b32 v126, 0xc0135761
v_mov_b32 v127, 0xc0135761
s_waitcnt vmcnt(0)
v_mov_b32 v18, s44
v_mov_b32 v19, s45
s_mov_b64 exec, 0xffff
buffer_store_dwordx2 v[18:19], v3, s[12:15], s77 offen sc1
s_mov_b64 exec, -1
s_add_u32 s77, s77, 6912
v_pk_fma_f32 v[48:49], v[96:97], v[114:115], v[48:49] op_sel_hi:[1,0,1]
v_pk_fma_f32 v[48:49], v[48:49], v[118:119], v[80:81] op_sel_hi:[1,0,1]
v_pk_mul_f32 v[120:121], v[48:49], v[48:49]
v_pk_fma_f32 v[120:121], v[120:121], v[124:125], v[126:127]
v_pk_mul_f32 v[120:121], v[120:121], v[48:49]
v_exp_f32 v120, v120
v_exp_f32 v121, v121
s_nop 0
v_pk_add_f32 v[120:121], v[120:121], 1.0 op_sel_hi:[1,0]
v_rcp_f32 v120, v120
v_rcp_f32 v121, v121
s_nop 0
v_pk_mul_f32 v[48:49], v[48:49], v[120:121]
v_cvt_pk_bf16_f32 v48, v48, v49
v_pk_fma_f32 v[50:51], v[98:99], v[114:115], v[50:51] op_sel_hi:[1,0,1]
v_pk_fma_f32 v[50:51], v[50:51], v[118:119], v[82:83] op_sel_hi:[1,0,1]
v_pk_mul_f32 v[122:123], v[50:51], v[50:51]
v_pk_fma_f32 v[122:123], v[122:123], v[124:125], v[126:127]
v_pk_mul_f32 v[122:123], v[122:123], v[50:51]
v_exp_f32 v122, v122
v_exp_f32 v123, v123
s_nop 0
v_pk_add_f32 v[122:123], v[122:123], 1.0 op_sel_hi:[1,0]
v_rcp_f32 v122, v122
v_rcp_f32 v123, v123
s_nop 0
v_pk_mul_f32 v[50:51], v[50:51], v[122:123]
v_cvt_pk_bf16_f32 v49, v50, v51
v_pk_fma_f32 v[52:53], v[100:101], v[114:115], v[52:53] op_sel_hi:[1,0,1]
v_pk_fma_f32 v[52:53], v[52:53], v[118:119], v[84:85] op_sel_hi:[1,0,1]
v_pk_mul_f32 v[120:121], v[52:53], v[52:53]
v_pk_fma_f32 v[120:121], v[120:121], v[124:125], v[126:127]
v_pk_mul_f32 v[120:121], v[120:121], v[52:53]
v_exp_f32 v120, v120
v_exp_f32 v121, v121
s_nop 0
v_pk_add_f32 v[120:121], v[120:121], 1.0 op_sel_hi:[1,0]
v_rcp_f32 v120, v120
v_rcp_f32 v121, v121
s_nop 0
v_pk_mul_f32 v[52:53], v[52:53], v[120:121]
v_cvt_pk_bf16_f32 v50, v52, v53
v_pk_fma_f32 v[54:55], v[102:103], v[114:115], v[54:55] op_sel_hi:[1,0,1]
v_pk_fma_f32 v[54:55], v[54:55], v[118:119], v[86:87] op_sel_hi:[1,0,1]
v_pk_mul_f32 v[122:123], v[54:55], v[54:55]
v_pk_fma_f32 v[122:123], v[122:123], v[124:125], v[126:127]
v_pk_mul_f32 v[122:123], v[122:123], v[54:55]
v_exp_f32 v122, v122
v_exp_f32 v123, v123
s_nop 0
v_pk_add_f32 v[122:123], v[122:123], 1.0 op_sel_hi:[1,0]
v_rcp_f32 v122, v122
v_rcp_f32 v123, v123
s_nop 0
v_pk_mul_f32 v[54:55], v[54:55], v[122:123]
v_cvt_pk_bf16_f32 v51, v54, v55
buffer_store_dwordx4 v[48:51], v28, s[12:15], s76 offen offset:0 sc1
v_pk_fma_f32 v[56:57], v[104:105], v[114:115], v[56:57] op_sel_hi:[1,0,1]
v_pk_fma_f32 v[56:57], v[56:57], v[118:119], v[88:89] op_sel_hi:[1,0,1]
v_pk_mul_f32 v[120:121], v[56:57], v[56:57]
v_pk_fma_f32 v[120:121], v[120:121], v[124:125], v[126:127]
v_pk_mul_f32 v[120:121], v[120:121], v[56:57]
v_exp_f32 v120, v120
v_exp_f32 v121, v121
s_nop 0
v_pk_add_f32 v[120:121], v[120:121], 1.0 op_sel_hi:[1,0]
v_rcp_f32 v120, v120
v_rcp_f32 v121, v121
s_nop 0
v_pk_mul_f32 v[56:57], v[56:57], v[120:121]
v_cvt_pk_bf16_f32 v56, v56, v57
v_pk_fma_f32 v[58:59], v[106:107], v[114:115], v[58:59] op_sel_hi:[1,0,1]
v_pk_fma_f32 v[58:59], v[58:59], v[118:119], v[90:91] op_sel_hi:[1,0,1]
v_pk_mul_f32 v[122:123], v[58:59], v[58:59]
v_pk_fma_f32 v[122:123], v[122:123], v[124:125], v[126:127]
v_pk_mul_f32 v[122:123], v[122:123], v[58:59]
v_exp_f32 v122, v122
v_exp_f32 v123, v123
s_nop 0
v_pk_add_f32 v[122:123], v[122:123], 1.0 op_sel_hi:[1,0]
v_rcp_f32 v122, v122
v_rcp_f32 v123, v123
s_nop 0
v_pk_mul_f32 v[58:59], v[58:59], v[122:123]
v_cvt_pk_bf16_f32 v57, v58, v59
v_pk_fma_f32 v[60:61], v[108:109], v[114:115], v[60:61] op_sel_hi:[1,0,1]
v_pk_fma_f32 v[60:61], v[60:61], v[118:119], v[92:93] op_sel_hi:[1,0,1]
v_pk_mul_f32 v[120:121], v[60:61], v[60:61]
v_pk_fma_f32 v[120:121], v[120:121], v[124:125], v[126:127]
v_pk_mul_f32 v[120:121], v[120:121], v[60:61]
v_exp_f32 v120, v120
v_exp_f32 v121, v121
s_nop 0
v_pk_add_f32 v[120:121], v[120:121], 1.0 op_sel_hi:[1,0]
v_rcp_f32 v120, v120
v_rcp_f32 v121, v121
s_nop 0
v_pk_mul_f32 v[60:61], v[60:61], v[120:121]
v_cvt_pk_bf16_f32 v58, v60, v61
v_pk_fma_f32 v[62:63], v[110:111], v[114:115], v[62:63] op_sel_hi:[1,0,1]
v_pk_fma_f32 v[62:63], v[62:63], v[118:119], v[94:95] op_sel_hi:[1,0,1]
v_pk_mul_f32 v[122:123], v[62:63], v[62:63]
v_pk_fma_f32 v[122:123], v[122:123], v[124:125], v[126:127]
v_pk_mul_f32 v[122:123], v[122:123], v[62:63]
v_exp_f32 v122, v122
v_exp_f32 v123, v123
s_nop 0
v_pk_add_f32 v[122:123], v[122:123], 1.0 op_sel_hi:[1,0]
v_rcp_f32 v122, v122
v_rcp_f32 v123, v123
s_nop 0
v_pk_mul_f32 v[62:63], v[62:63], v[122:123]
v_cvt_pk_bf16_f32 v59, v62, v63
buffer_store_dwordx4 v[56:59], v28, s[12:15], s76 offen offset:512 sc1
s_waitcnt vmcnt(0)
v_mov_b32 v18, s44
v_mov_b32 v19, s45
s_mov_b64 exec, 0xffff
buffer_store_dwordx2 v[18:19], v3, s[12:15], s77 offen sc1
s_mov_b64 exec, -1
s_branch .Lmk_f1join_41
.Lmk_nof1_40:
buffer_load_dwordx4 v[128:131], v2, s[12:15], s65 offen offset:2048 sc1
buffer_load_dwordx4 v[132:135], v2, s[12:15], s65 offen offset:3072 sc1
s_add_u32 s65, s65, 4096
buffer_load_dwordx4 v[136:139], v2, s[12:15], s65 offen offset:0 sc1
buffer_load_dwordx4 v[140:143], v2, s[12:15], s65 offen offset:1024 sc1
buffer_load_dwordx4 v[144:147], v2, s[12:15], s65 offen offset:2048 sc1
buffer_load_dwordx4 v[148:151], v2, s[12:15], s65 offen offset:3072 sc1
s_add_u32 s65, s65, 4096
buffer_load_dwordx4 v[152:155], v2, s[12:15], s65 offen offset:0 sc1
buffer_load_dwordx4 v[156:159], v2, s[12:15], s65 offen offset:1024 sc1
buffer_load_dwordx4 v[160:163], v2, s[12:15], s65 offen offset:2048 sc1
buffer_load_dwordx4 v[164:167], v2, s[12:15], s65 offen offset:3072 sc1
s_add_u32 s65, s65, 4096
buffer_load_dwordx4 v[168:171], v2, s[12:15], s65 offen offset:0 sc1
buffer_load_dwordx4 v[172:175], v2, s[12:15], s65 offen offset:1024 sc1
buffer_load_dwordx4 v[176:179], v2, s[12:15], s65 offen offset:2048 sc1
buffer_load_dwordx4 v[180:183], v2, s[12:15], s65 offen offset:3072 sc1
s_add_u32 s65, s65, 4096
buffer_load_dwordx4 v[184:187], v2, s[12:15], s65 offen offset:0 sc1
buffer_load_dwordx4 v[188:191], v2, s[12:15], s65 offen offset:1024 sc1
buffer_load_dwordx4 v[192:195], v2, s[12:15], s65 offen offset:2048 sc1
buffer_load_dwordx4 v[196:199], v2, s[12:15], s65 offen offset:3072 sc1
s_add_u32 s65, s65, 4096
buffer_load_dwordx4 v[200:203], v2, s[12:15], s65 offen offset:0 sc1
buffer_load_dwordx4 v[204:207], v2, s[12:15], s65 offen offset:1024 sc1
buffer_load_dwordx4 v[208:211], v2, s[12:15], s65 offen offset:2048 sc1
buffer_load_dwordx4 v[212:215], v2, s[12:15], s65 offen offset:3072 sc1
s_waitcnt vmcnt(0)
.Lmk_f1join_41:
v_lshrrev_b32 v16, 4, v0
s_mul_i32 s50, s89, 160
s_lshl_b32 s51, s71, 6
s_add_u32 s51, s51, s50
v_lshl_add_u32 v17, v16, 4, s51
buffer_load_dwordx4 v[26:29], v17, s[36:39], 0 offen
v_and_b32 v16, 1, v16
s_lshl_b32 s51, s71, 4
s_add_u32 s51, s51, s50
v_lshl_add_u32 v17, v16, 3, s51
buffer_load_dwordx2 v[30:31], v17, s[36:39], 0 offen offset:128
s_xor_b32 s50, s47, 1
s_mul_i32 s50, s50, 6144
s_add_u32 s50, s50, 2048
v_add_u32 v120, s50, v2
s_xor_b32 s50, s47, 2
s_mul_i32 s50, s50, 6144
s_add_u32 s50, s50, 4096
v_add_u32 v121, s50, v2
s_xor_b32 s50, s47, 3
s_mul_i32 s50, s50, 6144
s_add_u32 s50, s50, 6144
v_add_u32 v122, s50, v2
s_lshl_b32 s50, s47, 11
s_and_b32 s51, s47, 1
s_lshl_b32 s51, s51, 10
s_add_u32 s50, s50, s51
s_add_u32 s50, s50, 26624
v_add_u32 v123, s50, v2
s_lshl_b32 s50, s72, 1
s_add_u32 s50, s50, s90
s_mul_i32 s50, s50, 54
s_add_u32 s50, s50, s93
s_lshl_b32 s50, s50, 7
s_add_u32 s50, s50, 786432
s_cmp_ge_u32 s47, 2
s_cselect_b32 s51, 12, 13
v_min_u32 v16, s51, v0
v_lshlrev_b32 v16, 7, v16
s_add_u32 s51, s70, s90
s_lshl_b32 s51, s51, 1
s_and_b32 s52, s47, 1
s_xor_b32 s52, s52, 0
s_add_u32 s51, s51, s52
s_add_u32 s51, s51, 0
s_mul_i32 s51, s51, 55296
s_lshl_b32 s52, s93, 10
s_add_u32 s51, s51, s52
s_add_u32 s73, s51, 16777216
s_add_u32 s51, s70, s90
s_lshl_b32 s51, s51, 1
s_and_b32 s52, s47, 1
s_xor_b32 s52, s52, 1
s_add_u32 s51, s51, s52
s_add_u32 s51, s51, 0
)ASM"
      R"ASM(
s_mul_i32 s51, s51, 55296
s_lshl_b32 s52, s93, 10
s_add_u32 s51, s51, s52
s_add_u32 s74, s51, 16777216
.Lmk_poll_46:
buffer_load_dwordx2 v[18:19], v16, s[12:15], s50 offen sc1
s_waitcnt vmcnt(0)
v_cmp_ne_u64 vcc, s[44:45], v[18:19]
s_nop 0
s_cbranch_vccz .Lmk_polld_47
s_sub_u32 s46, s46, 1
s_cbranch_scc0 .Lmk_poll_46
s_mov_b32 s46, 0
.Lmk_polld_47:
buffer_load_dwordx4 a[56:59], v2, s[12:15], s73 offen offset:0 sc1
buffer_load_dwordx4 a[60:63], v2, s[12:15], s74 offen offset:0 sc1
buffer_load_dwordx4 a[64:67], v2, s[12:15], s73 offen offset:1024 sc1
buffer_load_dwordx4 a[68:71], v2, s[12:15], s74 offen offset:1024 sc1
buffer_load_dwordx4 a[72:75], v2, s[12:15], s73 offen offset:2048 sc1
buffer_load_dwordx4 a[76:79], v2, s[12:15], s74 offen offset:2048 sc1
buffer_load_dwordx4 a[80:83], v2, s[12:15], s73 offen offset:3072 sc1
s_add_u32 s73, s73, 4096
buffer_load_dwordx4 a[84:87], v2, s[12:15], s74 offen offset:3072 sc1
s_add_u32 s74, s74, 4096
buffer_load_dwordx4 a[88:91], v2, s[12:15], s73 offen offset:0 sc1
buffer_load_dwordx4 a[92:95], v2, s[12:15], s74 offen offset:0 sc1
s_waitcnt vmcnt(8)
v_mfma_f32_16x16x32_bf16 v[52:55], a[160:163], a[60:63], 0
v_mfma_f32_16x16x32_bf16 v[32:35], a[160:163], a[56:59], 0
buffer_load_dwordx4 a[96:99], v2, s[12:15], s73 offen offset:1024 sc1
v_mfma_f32_16x16x32_bf16 v[56:59], a[164:167], a[60:63], 0
v_mfma_f32_16x16x32_bf16 v[36:39], a[164:167], a[56:59], 0
v_mfma_f32_16x16x32_bf16 v[60:63], a[168:171], a[60:63], 0
v_mfma_f32_16x16x32_bf16 v[40:43], a[168:171], a[56:59], 0
buffer_load_dwordx4 a[100:103], v2, s[12:15], s74 offen offset:1024 sc1
v_mfma_f32_16x16x32_bf16 v[64:67], a[172:175], a[60:63], 0
v_mfma_f32_16x16x32_bf16 v[44:47], a[172:175], a[56:59], 0
v_mfma_f32_16x16x32_bf16 v[68:71], a[176:179], a[60:63], 0
v_mfma_f32_16x16x32_bf16 v[48:51], a[176:179], a[56:59], 0
buffer_load_dwordx4 a[104:107], v2, s[12:15], s73 offen offset:2048 sc1
s_waitcnt vmcnt(9)
v_mfma_f32_16x16x32_bf16 v[52:55], a[180:183], a[68:71], v[52:55]
v_mfma_f32_16x16x32_bf16 v[32:35], a[180:183], a[64:67], v[32:35]
buffer_load_dwordx4 a[108:111], v2, s[12:15], s74 offen offset:2048 sc1
v_mfma_f32_16x16x32_bf16 v[56:59], a[184:187], a[68:71], v[56:59]
v_mfma_f32_16x16x32_bf16 v[36:39], a[184:187], a[64:67], v[36:39]
v_mfma_f32_16x16x32_bf16 v[60:63], a[188:191], a[68:71], v[60:63]
v_mfma_f32_16x16x32_bf16 v[40:43], a[188:191], a[64:67], v[40:43]
buffer_load_dwordx4 a[112:115], v2, s[12:15], s73 offen offset:3072 sc1
s_add_u32 s73, s73, 4096
v_mfma_f32_16x16x32_bf16 v[64:67], a[192:195], a[68:71], v[64:67]
v_mfma_f32_16x16x32_bf16 v[44:47], a[192:195], a[64:67], v[44:47]
v_mfma_f32_16x16x32_bf16 v[68:71], a[196:199], a[68:71], v[68:71]
v_mfma_f32_16x16x32_bf16 v[48:51], a[196:199], a[64:67], v[48:51]
buffer_load_dwordx4 a[116:119], v2, s[12:15], s74 offen offset:3072 sc1
s_add_u32 s74, s74, 4096
s_waitcnt vmcnt(10)
v_mfma_f32_16x16x32_bf16 v[52:55], a[200:203], a[76:79], v[52:55]
v_mfma_f32_16x16x32_bf16 v[32:35], a[200:203], a[72:75], v[32:35]
s_add_u32 s65, s65, 4096
buffer_load_dwordx4 a[0:3], v2, s[12:15], s65 offen offset:0 sc1
v_mfma_f32_16x16x32_bf16 v[56:59], a[204:207], a[76:79], v[56:59]
v_mfma_f32_16x16x32_bf16 v[36:39], a[204:207], a[72:75], v[36:39]
v_mfma_f32_16x16x32_bf16 v[60:63], a[208:211], a[76:79], v[60:63]
v_mfma_f32_16x16x32_bf16 v[40:43], a[208:211], a[72:75], v[40:43]
buffer_load_dwordx4 a[120:123], v2, s[12:15], s73 offen offset:0 sc1
v_mfma_f32_16x16x32_bf16 v[64:67], a[212:215], a[76:79], v[64:67]
v_mfma_f32_16x16x32_bf16 v[44:47], a[212:215], a[72:75], v[44:47]
v_mfma_f32_16x16x32_bf16 v[68:71], a[216:219], a[76:79], v[68:71]
v_mfma_f32_16x16x32_bf16 v[48:51], a[216:219], a[72:75], v[48:51]
buffer_load_dwordx4 a[4:7], v2, s[12:15], s65 offen offset:1024 sc1
s_waitcnt vmcnt(11)
v_mfma_f32_16x16x32_bf16 v[52:55], a[220:223], a[84:87], v[52:55]
v_mfma_f32_16x16x32_bf16 v[32:35], a[220:223], a[80:83], v[32:35]
buffer_load_dwordx4 a[124:127], v2, s[12:15], s74 offen offset:0 sc1
v_mfma_f32_16x16x32_bf16 v[56:59], a[224:227], a[84:87], v[56:59]
v_mfma_f32_16x16x32_bf16 v[36:39], a[224:227], a[80:83], v[36:39]
v_mfma_f32_16x16x32_bf16 v[60:63], a[228:231], a[84:87], v[60:63]
v_mfma_f32_16x16x32_bf16 v[40:43], a[228:231], a[80:83], v[40:43]
buffer_load_dwordx4 a[8:11], v2, s[12:15], s65 offen offset:2048 sc1
v_mfma_f32_16x16x32_bf16 v[64:67], a[232:235], a[84:87], v[64:67]
v_mfma_f32_16x16x32_bf16 v[44:47], a[232:235], a[80:83], v[44:47]
v_mfma_f32_16x16x32_bf16 v[68:71], a[236:239], a[84:87], v[68:71]
v_mfma_f32_16x16x32_bf16 v[48:51], a[236:239], a[80:83], v[48:51]
buffer_load_dwordx4 a[128:131], v2, s[12:15], s73 offen offset:1024 sc1
s_waitcnt vmcnt(12)
v_mfma_f32_16x16x32_bf16 v[52:55], a[240:243], a[92:95], v[52:55]
v_mfma_f32_16x16x32_bf16 v[32:35], a[240:243], a[88:91], v[32:35]
buffer_load_dwordx4 a[12:15], v2, s[12:15], s65 offen offset:3072 sc1
v_mfma_f32_16x16x32_bf16 v[56:59], a[244:247], a[92:95], v[56:59]
v_mfma_f32_16x16x32_bf16 v[36:39], a[244:247], a[88:91], v[36:39]
v_mfma_f32_16x16x32_bf16 v[60:63], a[248:251], a[92:95], v[60:63]
v_mfma_f32_16x16x32_bf16 v[40:43], a[248:251], a[88:91], v[40:43]
buffer_load_dwordx4 a[132:135], v2, s[12:15], s74 offen offset:1024 sc1
v_mfma_f32_16x16x32_bf16 v[64:67], a[252:255], a[92:95], v[64:67]
v_mfma_f32_16x16x32_bf16 v[44:47], a[252:255], a[88:91], v[44:47]
v_mfma_f32_16x16x32_bf16 v[68:71], v[216:219], a[92:95], v[68:71]
v_mfma_f32_16x16x32_bf16 v[48:51], v[216:219], a[88:91], v[48:51]
s_add_u32 s65, s65, 4096
buffer_load_dwordx4 a[16:19], v2, s[12:15], s65 offen offset:0 sc1
s_waitcnt vmcnt(13)
v_mfma_f32_16x16x32_bf16 v[52:55], v[220:223], a[100:103], v[52:55]
v_mfma_f32_16x16x32_bf16 v[32:35], v[220:223], a[96:99], v[32:35]
buffer_load_dwordx4 a[136:139], v2, s[12:15], s73 offen offset:2048 sc1
v_mfma_f32_16x16x32_bf16 v[56:59], v[224:227], a[100:103], v[56:59]
v_mfma_f32_16x16x32_bf16 v[36:39], v[224:227], a[96:99], v[36:39]
v_mfma_f32_16x16x32_bf16 v[60:63], v[228:231], a[100:103], v[60:63]
v_mfma_f32_16x16x32_bf16 v[40:43], v[228:231], a[96:99], v[40:43]
buffer_load_dwordx4 a[20:23], v2, s[12:15], s65 offen offset:1024 sc1
v_mfma_f32_16x16x32_bf16 v[64:67], v[232:235], a[100:103], v[64:67]
v_mfma_f32_16x16x32_bf16 v[44:47], v[232:235], a[96:99], v[44:47]
v_mfma_f32_16x16x32_bf16 v[68:71], v[236:239], a[100:103], v[68:71]
v_mfma_f32_16x16x32_bf16 v[48:51], v[236:239], a[96:99], v[48:51]
buffer_load_dwordx4 a[140:143], v2, s[12:15], s74 offen offset:2048 sc1
s_waitcnt vmcnt(14)
v_mfma_f32_16x16x32_bf16 v[52:55], v[240:243], a[108:111], v[52:55]
v_mfma_f32_16x16x32_bf16 v[32:35], v[240:243], a[104:107], v[32:35]
buffer_load_dwordx4 a[24:27], v2, s[12:15], s65 offen offset:2048 sc1
v_mfma_f32_16x16x32_bf16 v[56:59], v[244:247], a[108:111], v[56:59]
v_mfma_f32_16x16x32_bf16 v[36:39], v[244:247], a[104:107], v[36:39]
v_mfma_f32_16x16x32_bf16 v[60:63], v[248:251], a[108:111], v[60:63]
v_mfma_f32_16x16x32_bf16 v[40:43], v[248:251], a[104:107], v[40:43]
buffer_load_dwordx4 a[144:147], v2, s[12:15], s73 offen offset:3072 sc1
s_add_u32 s73, s73, 4096
v_mfma_f32_16x16x32_bf16 v[64:67], v[252:255], a[108:111], v[64:67]
v_mfma_f32_16x16x32_bf16 v[44:47], v[252:255], a[104:107], v[44:47]
v_mfma_f32_16x16x32_bf16 v[68:71], v[128:131], a[108:111], v[68:71]
v_mfma_f32_16x16x32_bf16 v[48:51], v[128:131], a[104:107], v[48:51]
buffer_load_dwordx4 a[28:31], v2, s[12:15], s65 offen offset:3072 sc1
s_waitcnt vmcnt(15)
v_mfma_f32_16x16x32_bf16 v[52:55], v[132:135], a[116:119], v[52:55]
v_mfma_f32_16x16x32_bf16 v[32:35], v[132:135], a[112:115], v[32:35]
buffer_load_dwordx4 a[148:151], v2, s[12:15], s74 offen offset:3072 sc1
s_add_u32 s74, s74, 4096
v_mfma_f32_16x16x32_bf16 v[56:59], v[136:139], a[116:119], v[56:59]
v_mfma_f32_16x16x32_bf16 v[36:39], v[136:139], a[112:115], v[36:39]
v_mfma_f32_16x16x32_bf16 v[60:63], v[140:143], a[116:119], v[60:63]
v_mfma_f32_16x16x32_bf16 v[40:43], v[140:143], a[112:115], v[40:43]
s_add_u32 s65, s65, 4096
buffer_load_dwordx4 a[32:35], v2, s[12:15], s65 offen offset:0 sc1
v_mfma_f32_16x16x32_bf16 v[64:67], v[144:147], a[116:119], v[64:67]
v_mfma_f32_16x16x32_bf16 v[44:47], v[144:147], a[112:115], v[44:47]
v_mfma_f32_16x16x32_bf16 v[68:71], v[148:151], a[116:119], v[68:71]
v_mfma_f32_16x16x32_bf16 v[48:51], v[148:151], a[112:115], v[48:51]
buffer_load_dwordx4 a[152:155], v2, s[12:15], s73 offen offset:0 sc1
s_waitcnt vmcnt(14)
v_mfma_f32_16x16x32_bf16 v[52:55], v[152:155], a[124:127], v[52:55]
v_mfma_f32_16x16x32_bf16 v[32:35], v[152:155], a[120:123], v[32:35]
buffer_load_dwordx4 a[36:39], v2, s[12:15], s65 offen offset:1024 sc1
v_mfma_f32_16x16x32_bf16 v[56:59], v[156:159], a[124:127], v[56:59]
v_mfma_f32_16x16x32_bf16 v[36:39], v[156:159], a[120:123], v[36:39]
v_mfma_f32_16x16x32_bf16 v[60:63], v[160:163], a[124:127], v[60:63]
v_mfma_f32_16x16x32_bf16 v[40:43], v[160:163], a[120:123], v[40:43]
buffer_load_dwordx4 a[156:159], v2, s[12:15], s74 offen offset:0 sc1
v_mfma_f32_16x16x32_bf16 v[64:67], v[164:167], a[124:127], v[64:67]
v_mfma_f32_16x16x32_bf16 v[44:47], v[164:167], a[120:123], v[44:47]
v_mfma_f32_16x16x32_bf16 v[68:71], v[168:171], a[124:127], v[68:71]
v_mfma_f32_16x16x32_bf16 v[48:51], v[168:171], a[120:123], v[48:51]
buffer_load_dwordx4 a[40:43], v2, s[12:15], s65 offen offset:2048 sc1
s_waitcnt vmcnt(13)
v_mfma_f32_16x16x32_bf16 v[52:55], v[172:175], a[132:135], v[52:55]
v_mfma_f32_16x16x32_bf16 v[32:35], v[172:175], a[128:131], v[32:35]
buffer_load_dwordx4 v[112:115], v2, s[12:15], s73 offen offset:1024 sc1
v_mfma_f32_16x16x32_bf16 v[56:59], v[176:179], a[132:135], v[56:59]
v_mfma_f32_16x16x32_bf16 v[36:39], v[176:179], a[128:131], v[36:39]
v_mfma_f32_16x16x32_bf16 v[60:63], v[180:183], a[132:135], v[60:63]
v_mfma_f32_16x16x32_bf16 v[40:43], v[180:183], a[128:131], v[40:43]
buffer_load_dwordx4 a[44:47], v2, s[12:15], s65 offen offset:3072 sc1
v_mfma_f32_16x16x32_bf16 v[64:67], v[184:187], a[132:135], v[64:67]
v_mfma_f32_16x16x32_bf16 v[44:47], v[184:187], a[128:131], v[44:47]
v_mfma_f32_16x16x32_bf16 v[68:71], v[188:191], a[132:135], v[68:71]
v_mfma_f32_16x16x32_bf16 v[48:51], v[188:191], a[128:131], v[48:51]
buffer_load_dwordx4 v[116:119], v2, s[12:15], s74 offen offset:1024 sc1
s_waitcnt vmcnt(12)
v_mfma_f32_16x16x32_bf16 v[52:55], v[192:195], a[140:143], v[52:55]
v_mfma_f32_16x16x32_bf16 v[32:35], v[192:195], a[136:139], v[32:35]
s_add_u32 s65, s65, 4096
buffer_load_dwordx4 a[48:51], v2, s[12:15], s65 offen offset:0 sc1
v_mfma_f32_16x16x32_bf16 v[56:59], v[196:199], a[140:143], v[56:59]
v_mfma_f32_16x16x32_bf16 v[36:39], v[196:199], a[136:139], v[36:39]
v_mfma_f32_16x16x32_bf16 v[60:63], v[200:203], a[140:143], v[60:63]
v_mfma_f32_16x16x32_bf16 v[40:43], v[200:203], a[136:139], v[40:43]
buffer_load_dwordx4 a[52:55], v2, s[12:15], s65 offen offset:1024 sc1
v_mfma_f32_16x16x32_bf16 v[64:67], v[204:207], a[140:143], v[64:67]
v_mfma_f32_16x16x32_bf16 v[44:47], v[204:207], a[136:139], v[44:47]
v_mfma_f32_16x16x32_bf16 v[68:71], v[208:211], a[140:143], v[68:71]
v_mfma_f32_16x16x32_bf16 v[48:51], v[208:211], a[136:139], v[48:51]
s_waitcnt vmcnt(10)
v_mfma_f32_16x16x32_bf16 v[52:55], v[212:215], a[148:151], v[52:55]
v_mfma_f32_16x16x32_bf16 v[32:35], v[212:215], a[144:147], v[32:35]
v_mfma_f32_16x16x32_bf16 v[56:59], a[0:3], a[148:151], v[56:59]
v_mfma_f32_16x16x32_bf16 v[36:39], a[0:3], a[144:147], v[36:39]
v_mfma_f32_16x16x32_bf16 v[60:63], a[4:7], a[148:151], v[60:63]
v_mfma_f32_16x16x32_bf16 v[40:43], a[4:7], a[144:147], v[40:43]
v_mfma_f32_16x16x32_bf16 v[64:67], a[8:11], a[148:151], v[64:67]
v_mfma_f32_16x16x32_bf16 v[44:47], a[8:11], a[144:147], v[44:47]
v_mfma_f32_16x16x32_bf16 v[68:71], a[12:15], a[148:151], v[68:71]
v_mfma_f32_16x16x32_bf16 v[48:51], a[12:15], a[144:147], v[48:51]
s_waitcnt vmcnt(6)
v_mfma_f32_16x16x32_bf16 v[52:55], a[16:19], a[156:159], v[52:55]
v_mfma_f32_16x16x32_bf16 v[32:35], a[16:19], a[152:155], v[32:35]
v_mfma_f32_16x16x32_bf16 v[56:59], a[20:23], a[156:159], v[56:59]
v_mfma_f32_16x16x32_bf16 v[36:39], a[20:23], a[152:155], v[36:39]
v_mfma_f32_16x16x32_bf16 v[60:63], a[24:27], a[156:159], v[60:63]
v_mfma_f32_16x16x32_bf16 v[40:43], a[24:27], a[152:155], v[40:43]
v_mfma_f32_16x16x32_bf16 v[64:67], a[28:31], a[156:159], v[64:67]
v_mfma_f32_16x16x32_bf16 v[44:47], a[28:31], a[152:155], v[44:47]
v_mfma_f32_16x16x32_bf16 v[68:71], a[32:35], a[156:159], v[68:71]
v_mfma_f32_16x16x32_bf16 v[48:51], a[32:35], a[152:155], v[48:51]
s_cmp_ge_u32 s47, 2
s_waitcnt vmcnt(0)
s_cbranch_scc1 .Lmk_f2skip_48
v_mfma_f32_16x16x32_bf16 v[52:55], a[36:39], v[116:119], v[52:55]
v_mfma_f32_16x16x32_bf16 v[32:35], a[36:39], v[112:115], v[32:35]
v_mfma_f32_16x16x32_bf16 v[56:59], a[40:43], v[116:119], v[56:59]
v_mfma_f32_16x16x32_bf16 v[36:39], a[40:43], v[112:115], v[36:39]
v_mfma_f32_16x16x32_bf16 v[60:63], a[44:47], v[116:119], v[60:63]
v_mfma_f32_16x16x32_bf16 v[40:43], a[44:47], v[112:115], v[40:43]
v_mfma_f32_16x16x32_bf16 v[64:67], a[48:51], v[116:119], v[64:67]
v_mfma_f32_16x16x32_bf16 v[44:47], a[48:51], v[112:115], v[44:47]
v_mfma_f32_16x16x32_bf16 v[68:71], a[52:55], v[116:119], v[68:71]
v_mfma_f32_16x16x32_bf16 v[48:51], a[52:55], v[112:115], v[48:51]
.Lmk_f2skip_48:
s_nop 4
v_xor_b32 v16, 1024, v123
ds_write_b128 v120, v[52:55] offset:0
ds_write_b128 v120, v[56:59] offset:1024
ds_write_b128 v122, v[60:63] offset:0
ds_write_b128 v122, v[64:67] offset:1024
ds_write_b128 v121, v[40:43] offset:0
ds_write_b128 v121, v[44:47] offset:1024
ds_write_b128 v16, v[68:71]
ds_write_b128 v123, v[48:51]
s_mul_i32 s50, s47, 6144
s_add_u32 s50, s50, 2048
v_add_u32 v16, s50, v2
s_and_b32 s51, s47, 1
s_lshl_b32 s51, s51, 10
s_lshl_b32 s52, s71, 9
s_add_u32 s51, s51, s52
s_add_u32 s51, s51, 26624
v_and_b32 v17, 496, v2
v_add_u32 v17, s51, v17
s_waitcnt lgkmcnt(0)
s_barrier
ds_read_b128 v[72:75], v16 offset:0
ds_read_b128 v[76:79], v16 offset:1024
ds_read_b128 v[80:83], v16 offset:2048
ds_read_b128 v[84:87], v16 offset:3072
ds_read_b128 v[88:91], v16 offset:4096
ds_read_b128 v[92:95], v16 offset:5120
ds_read_b128 v[96:99], v17 offset:0
ds_read_b128 v[100:103], v17 offset:2048
ds_read_b128 v[104:107], v17 offset:4096
ds_read_b128 v[108:111], v17 offset:6144
s_waitcnt lgkmcnt(8)
v_pk_add_f32 v[32:33], v[32:33], v[72:73]
v_pk_add_f32 v[34:35], v[34:35], v[74:75]
v_pk_add_f32 v[36:37], v[36:37], v[76:77]
v_pk_add_f32 v[38:39], v[38:39], v[78:79]
s_waitcnt lgkmcnt(6)
v_pk_add_f32 v[32:33], v[32:33], v[80:81]
v_pk_add_f32 v[34:35], v[34:35], v[82:83]
v_pk_add_f32 v[36:37], v[36:37], v[84:85]
v_pk_add_f32 v[38:39], v[38:39], v[86:87]
s_waitcnt lgkmcnt(4)
v_pk_add_f32 v[32:33], v[32:33], v[88:89]
v_pk_add_f32 v[34:35], v[34:35], v[90:91]
v_pk_add_f32 v[36:37], v[36:37], v[92:93]
v_pk_add_f32 v[38:39], v[38:39], v[94:95]
s_waitcnt lgkmcnt(2)
v_pk_add_f32 v[96:97], v[96:97], v[100:101]
v_pk_add_f32 v[98:99], v[98:99], v[102:103]
s_waitcnt lgkmcnt(0)
v_pk_add_f32 v[104:105], v[104:105], v[108:109]
v_pk_add_f32 v[106:107], v[106:107], v[110:111]
v_pk_add_f32 v[96:97], v[96:97], v[104:105]
v_pk_add_f32 v[98:99], v[98:99], v[106:107]
v_lshlrev_b32 v52, 16, v4
v_and_b32 v53, 0xffff0000, v4
v_pk_add_f32 v[32:33], v[32:33], v[52:53]
v_lshlrev_b32 v52, 16, v5
v_and_b32 v53, 0xffff0000, v5
v_pk_add_f32 v[34:35], v[34:35], v[52:53]
v_lshlrev_b32 v52, 16, v6
v_and_b32 v53, 0xffff0000, v6
v_pk_add_f32 v[36:37], v[36:37], v[52:53]
v_lshlrev_b32 v52, 16, v7
v_and_b32 v53, 0xffff0000, v7
v_pk_add_f32 v[38:39], v[38:39], v[52:53]
v_lshlrev_b32 v52, 16, v8
v_and_b32 v53, 0xffff0000, v8
v_pk_add_f32 v[96:97], v[96:97], v[52:53]
v_lshlrev_b32 v52, 16, v9
v_and_b32 v53, 0xffff0000, v9
v_pk_add_f32 v[98:99], v[98:99], v[52:53]
v_lshlrev_b32 v52, 16, v26
v_and_b32 v53, 0xffff0000, v26
v_pk_add_f32 v[32:33], v[32:33], v[52:53]
v_lshlrev_b32 v52, 16, v27
v_and_b32 v53, 0xffff0000, v27
v_pk_add_f32 v[34:35], v[34:35], v[52:53]
v_lshlrev_b32 v52, 16, v28
v_and_b32 v53, 0xffff0000, v28
v_pk_add_f32 v[36:37], v[36:37], v[52:53]
v_lshlrev_b32 v52, 16, v29
v_and_b32 v53, 0xffff0000, v29
v_pk_add_f32 v[38:39], v[38:39], v[52:53]
v_lshlrev_b32 v52, 16, v30
v_and_b32 v53, 0xffff0000, v30
v_pk_add_f32 v[96:97], v[96:97], v[52:53]
v_lshlrev_b32 v52, 16, v31
v_and_b32 v53, 0xffff0000, v31
v_pk_add_f32 v[98:99], v[98:99], v[52:53]
v_cvt_pk_bf16_f32 v56, v32, v33
v_cvt_pk_bf16_f32 v57, v34, v35
v_cvt_pk_bf16_f32 v58, v36, v37
v_cvt_pk_bf16_f32 v59, v38, v39
v_cvt_pk_bf16_f32 v60, v96, v97
v_cvt_pk_bf16_f32 v61, v98, v99
buffer_store_dwordx4 v[56:59], v20, s[16:19], 0 offen
buffer_store_dwordx2 v[60:61], v21, s[16:19], 0 offen
s_xor_b32 s50, s47, 1
s_mul_i32 s50, s50, 6144
s_add_u32 s50, s50, 34816
v_add_u32 v120, s50, v2
s_xor_b32 s50, s47, 2
s_mul_i32 s50, s50, 6144
s_add_u32 s50, s50, 36864
v_add_u32 v121, s50, v2
s_xor_b32 s50, s47, 3
s_mul_i32 s50, s50, 6144
s_add_u32 s50, s50, 38912
v_add_u32 v122, s50, v2
s_lshl_b32 s50, s47, 11
s_and_b32 s51, s47, 1
s_lshl_b32 s51, s51, 10
s_add_u32 s50, s50, s51
s_add_u32 s50, s50, 59392
v_add_u32 v123, s50, v2
s_lshl_b32 s50, s72, 1
s_add_u32 s50, s50, s90
s_mul_i32 s50, s50, 54
s_add_u32 s50, s50, s93
s_lshl_b32 s50, s50, 7
s_add_u32 s50, s50, 1007616
s_cmp_ge_u32 s47, 2
s_cselect_b32 s51, 12, 13
v_min_u32 v16, s51, v0
v_lshlrev_b32 v16, 7, v16
s_add_u32 s51, s70, s90
s_lshl_b32 s51, s51, 1
s_and_b32 s52, s47, 1
s_xor_b32 s52, s52, 0
s_add_u32 s51, s51, s52
s_add_u32 s51, s51, 8
s_mul_i32 s51, s51, 55296
s_lshl_b32 s52, s93, 10
s_add_u32 s51, s51, s52
s_add_u32 s73, s51, 16777216
s_add_u32 s51, s70, s90
s_lshl_b32 s51, s51, 1
s_and_b32 s52, s47, 1
s_xor_b32 s52, s52, 1
s_add_u32 s51, s51, s52
s_add_u32 s51, s51, 8
s_mul_i32 s51, s51, 55296
s_lshl_b32 s52, s93, 10
s_add_u32 s51, s51, s52
s_add_u32 s74, s51, 16777216
.Lmk_poll_49:
buffer_load_dwordx2 v[18:19], v16, s[12:15], s50 offen sc1
s_waitcnt vmcnt(0)
v_cmp_ne_u64 vcc, s[44:45], v[18:19]
s_nop 0
s_cbranch_vccz .Lmk_polld_50
s_sub_u32 s46, s46, 1
s_cbranch_scc0 .Lmk_poll_49
s_mov_b32 s46, 0
.Lmk_polld_50:
buffer_load_dwordx4 a[56:59], v2, s[12:15], s73 offen offset:0 sc1
buffer_load_dwordx4 a[60:63], v2, s[12:15], s74 offen offset:0 sc1
buffer_load_dwordx4 a[64:67], v2, s[12:15], s73 offen offset:1024 sc1
buffer_load_dwordx4 a[68:71], v2, s[12:15], s74 offen offset:1024 sc1
buffer_load_dwordx4 a[72:75], v2, s[12:15], s73 offen offset:2048 sc1
buffer_load_dwordx4 a[76:79], v2, s[12:15], s74 offen offset:2048 sc1
buffer_load_dwordx4 a[80:83], v2, s[12:15], s73 offen offset:3072 sc1
s_add_u32 s73, s73, 4096
buffer_load_dwordx4 a[84:87], v2, s[12:15], s74 offen offset:3072 sc1
s_add_u32 s74, s74, 4096
buffer_load_dwordx4 a[88:91], v2, s[12:15], s73 offen offset:0 sc1
buffer_load_dwordx4 a[92:95], v2, s[12:15], s74 offen offset:0 sc1
s_waitcnt vmcnt(8)
v_mfma_f32_16x16x32_bf16 v[52:55], a[160:163], a[60:63], 0
v_mfma_f32_16x16x32_bf16 v[32:35], a[160:163], a[56:59], 0
buffer_load_dwordx4 a[96:99], v2, s[12:15], s73 offen offset:1024 sc1
v_mfma_f32_16x16x32_bf16 v[56:59], a[164:167], a[60:63], 0
v_mfma_f32_16x16x32_bf16 v[36:39], a[164:167], a[56:59], 0
v_mfma_f32_16x16x32_bf16 v[60:63], a[168:171], a[60:63], 0
v_mfma_f32_16x16x32_bf16 v[40:43], a[168:171], a[56:59], 0
)ASM"
      R"ASM(
buffer_load_dwordx4 a[100:103], v2, s[12:15], s74 offen offset:1024 sc1
v_mfma_f32_16x16x32_bf16 v[64:67], a[172:175], a[60:63], 0
v_mfma_f32_16x16x32_bf16 v[44:47], a[172:175], a[56:59], 0
v_mfma_f32_16x16x32_bf16 v[68:71], a[176:179], a[60:63], 0
v_mfma_f32_16x16x32_bf16 v[48:51], a[176:179], a[56:59], 0
s_waitcnt vmcnt(8)
v_mfma_f32_16x16x32_bf16 v[52:55], a[180:183], a[68:71], v[52:55]
v_mfma_f32_16x16x32_bf16 v[32:35], a[180:183], a[64:67], v[32:35]
buffer_load_dwordx4 a[104:107], v2, s[12:15], s73 offen offset:2048 sc1
v_mfma_f32_16x16x32_bf16 v[56:59], a[184:187], a[68:71], v[56:59]
v_mfma_f32_16x16x32_bf16 v[36:39], a[184:187], a[64:67], v[36:39]
v_mfma_f32_16x16x32_bf16 v[60:63], a[188:191], a[68:71], v[60:63]
v_mfma_f32_16x16x32_bf16 v[40:43], a[188:191], a[64:67], v[40:43]
buffer_load_dwordx4 a[108:111], v2, s[12:15], s74 offen offset:2048 sc1
v_mfma_f32_16x16x32_bf16 v[64:67], a[192:195], a[68:71], v[64:67]
v_mfma_f32_16x16x32_bf16 v[44:47], a[192:195], a[64:67], v[44:47]
v_mfma_f32_16x16x32_bf16 v[68:71], a[196:199], a[68:71], v[68:71]
v_mfma_f32_16x16x32_bf16 v[48:51], a[196:199], a[64:67], v[48:51]
s_waitcnt vmcnt(8)
v_mfma_f32_16x16x32_bf16 v[52:55], a[200:203], a[76:79], v[52:55]
v_mfma_f32_16x16x32_bf16 v[32:35], a[200:203], a[72:75], v[32:35]
buffer_load_dwordx4 a[112:115], v2, s[12:15], s73 offen offset:3072 sc1
s_add_u32 s73, s73, 4096
v_mfma_f32_16x16x32_bf16 v[56:59], a[204:207], a[76:79], v[56:59]
v_mfma_f32_16x16x32_bf16 v[36:39], a[204:207], a[72:75], v[36:39]
v_mfma_f32_16x16x32_bf16 v[60:63], a[208:211], a[76:79], v[60:63]
v_mfma_f32_16x16x32_bf16 v[40:43], a[208:211], a[72:75], v[40:43]
buffer_load_dwordx4 a[116:119], v2, s[12:15], s74 offen offset:3072 sc1
s_add_u32 s74, s74, 4096
v_mfma_f32_16x16x32_bf16 v[64:67], a[212:215], a[76:79], v[64:67]
v_mfma_f32_16x16x32_bf16 v[44:47], a[212:215], a[72:75], v[44:47]
v_mfma_f32_16x16x32_bf16 v[68:71], a[216:219], a[76:79], v[68:71]
v_mfma_f32_16x16x32_bf16 v[48:51], a[216:219], a[72:75], v[48:51]
s_waitcnt vmcnt(8)
v_mfma_f32_16x16x32_bf16 v[52:55], a[220:223], a[84:87], v[52:55]
v_mfma_f32_16x16x32_bf16 v[32:35], a[220:223], a[80:83], v[32:35]
buffer_load_dwordx4 a[120:123], v2, s[12:15], s73 offen offset:0 sc1
v_mfma_f32_16x16x32_bf16 v[56:59], a[224:227], a[84:87], v[56:59]
v_mfma_f32_16x16x32_bf16 v[36:39], a[224:227], a[80:83], v[36:39]
v_mfma_f32_16x16x32_bf16 v[60:63], a[228:231], a[84:87], v[60:63]
v_mfma_f32_16x16x32_bf16 v[40:43], a[228:231], a[80:83], v[40:43]
buffer_load_dwordx4 a[124:127], v2, s[12:15], s74 offen offset:0 sc1
v_mfma_f32_16x16x32_bf16 v[64:67], a[232:235], a[84:87], v[64:67]
v_mfma_f32_16x16x32_bf16 v[44:47], a[232:235], a[80:83], v[44:47]
v_mfma_f32_16x16x32_bf16 v[68:71], a[236:239], a[84:87], v[68:71]
v_mfma_f32_16x16x32_bf16 v[48:51], a[236:239], a[80:83], v[48:51]
s_waitcnt vmcnt(8)
v_mfma_f32_16x16x32_bf16 v[52:55], a[240:243], a[92:95], v[52:55]
v_mfma_f32_16x16x32_bf16 v[32:35], a[240:243], a[88:91], v[32:35]
buffer_load_dwordx4 a[128:131], v2, s[12:15], s73 offen offset:1024 sc1
v_mfma_f32_16x16x32_bf16 v[56:59], a[244:247], a[92:95], v[56:59]
v_mfma_f32_16x16x32_bf16 v[36:39], a[244:247], a[88:91], v[36:39]
v_mfma_f32_16x16x32_bf16 v[60:63], a[248:251], a[92:95], v[60:63]
v_mfma_f32_16x16x32_bf16 v[40:43], a[248:251], a[88:91], v[40:43]
buffer_load_dwordx4 a[132:135], v2, s[12:15], s74 offen offset:1024 sc1
v_mfma_f32_16x16x32_bf16 v[64:67], a[252:255], a[92:95], v[64:67]
v_mfma_f32_16x16x32_bf16 v[44:47], a[252:255], a[88:91], v[44:47]
v_mfma_f32_16x16x32_bf16 v[68:71], v[216:219], a[92:95], v[68:71]
v_mfma_f32_16x16x32_bf16 v[48:51], v[216:219], a[88:91], v[48:51]
s_waitcnt vmcnt(8)
v_mfma_f32_16x16x32_bf16 v[52:55], v[220:223], a[100:103], v[52:55]
v_mfma_f32_16x16x32_bf16 v[32:35], v[220:223], a[96:99], v[32:35]
buffer_load_dwordx4 a[136:139], v2, s[12:15], s73 offen offset:2048 sc1
v_mfma_f32_16x16x32_bf16 v[56:59], v[224:227], a[100:103], v[56:59]
v_mfma_f32_16x16x32_bf16 v[36:39], v[224:227], a[96:99], v[36:39]
v_mfma_f32_16x16x32_bf16 v[60:63], v[228:231], a[100:103], v[60:63]
v_mfma_f32_16x16x32_bf16 v[40:43], v[228:231], a[96:99], v[40:43]
buffer_load_dwordx4 a[140:143], v2, s[12:15], s74 offen offset:2048 sc1
v_mfma_f32_16x16x32_bf16 v[64:67], v[232:235], a[100:103], v[64:67]
v_mfma_f32_16x16x32_bf16 v[44:47], v[232:235], a[96:99], v[44:47]
v_mfma_f32_16x16x32_bf16 v[68:71], v[236:239], a[100:103], v[68:71]
v_mfma_f32_16x16x32_bf16 v[48:51], v[236:239], a[96:99], v[48:51]
s_waitcnt vmcnt(8)
v_mfma_f32_16x16x32_bf16 v[52:55], v[240:243], a[108:111], v[52:55]
v_mfma_f32_16x16x32_bf16 v[32:35], v[240:243], a[104:107], v[32:35]
buffer_load_dwordx4 a[144:147], v2, s[12:15], s73 offen offset:3072 sc1
s_add_u32 s73, s73, 4096
v_mfma_f32_16x16x32_bf16 v[56:59], v[244:247], a[108:111], v[56:59]
v_mfma_f32_16x16x32_bf16 v[36:39], v[244:247], a[104:107], v[36:39]
v_mfma_f32_16x16x32_bf16 v[60:63], v[248:251], a[108:111], v[60:63]
v_mfma_f32_16x16x32_bf16 v[40:43], v[248:251], a[104:107], v[40:43]
buffer_load_dwordx4 a[148:151], v2, s[12:15], s74 offen offset:3072 sc1
s_add_u32 s74, s74, 4096
v_mfma_f32_16x16x32_bf16 v[64:67], v[252:255], a[108:111], v[64:67]
v_mfma_f32_16x16x32_bf16 v[44:47], v[252:255], a[104:107], v[44:47]
v_mfma_f32_16x16x32_bf16 v[68:71], v[128:131], a[108:111], v[68:71]
v_mfma_f32_16x16x32_bf16 v[48:51], v[128:131], a[104:107], v[48:51]
s_waitcnt vmcnt(8)
v_mfma_f32_16x16x32_bf16 v[52:55], v[132:135], a[116:119], v[52:55]
v_mfma_f32_16x16x32_bf16 v[32:35], v[132:135], a[112:115], v[32:35]
buffer_load_dwordx4 a[152:155], v2, s[12:15], s73 offen offset:0 sc1
v_mfma_f32_16x16x32_bf16 v[56:59], v[136:139], a[116:119], v[56:59]
v_mfma_f32_16x16x32_bf16 v[36:39], v[136:139], a[112:115], v[36:39]
v_mfma_f32_16x16x32_bf16 v[60:63], v[140:143], a[116:119], v[60:63]
v_mfma_f32_16x16x32_bf16 v[40:43], v[140:143], a[112:115], v[40:43]
buffer_load_dwordx4 a[156:159], v2, s[12:15], s74 offen offset:0 sc1
v_mfma_f32_16x16x32_bf16 v[64:67], v[144:147], a[116:119], v[64:67]
v_mfma_f32_16x16x32_bf16 v[44:47], v[144:147], a[112:115], v[44:47]
v_mfma_f32_16x16x32_bf16 v[68:71], v[148:151], a[116:119], v[68:71]
v_mfma_f32_16x16x32_bf16 v[48:51], v[148:151], a[112:115], v[48:51]
s_waitcnt vmcnt(8)
v_mfma_f32_16x16x32_bf16 v[52:55], v[152:155], a[124:127], v[52:55]
v_mfma_f32_16x16x32_bf16 v[32:35], v[152:155], a[120:123], v[32:35]
buffer_load_dwordx4 v[112:115], v2, s[12:15], s73 offen offset:1024 sc1
v_mfma_f32_16x16x32_bf16 v[56:59], v[156:159], a[124:127], v[56:59]
v_mfma_f32_16x16x32_bf16 v[36:39], v[156:159], a[120:123], v[36:39]
v_mfma_f32_16x16x32_bf16 v[60:63], v[160:163], a[124:127], v[60:63]
v_mfma_f32_16x16x32_bf16 v[40:43], v[160:163], a[120:123], v[40:43]
buffer_load_dwordx4 v[116:119], v2, s[12:15], s74 offen offset:1024 sc1
v_mfma_f32_16x16x32_bf16 v[64:67], v[164:167], a[124:127], v[64:67]
v_mfma_f32_16x16x32_bf16 v[44:47], v[164:167], a[120:123], v[44:47]
v_mfma_f32_16x16x32_bf16 v[68:71], v[168:171], a[124:127], v[68:71]
v_mfma_f32_16x16x32_bf16 v[48:51], v[168:171], a[120:123], v[48:51]
s_waitcnt vmcnt(8)
v_mfma_f32_16x16x32_bf16 v[52:55], v[172:175], a[132:135], v[52:55]
v_mfma_f32_16x16x32_bf16 v[32:35], v[172:175], a[128:131], v[32:35]
v_mfma_f32_16x16x32_bf16 v[56:59], v[176:179], a[132:135], v[56:59]
v_mfma_f32_16x16x32_bf16 v[36:39], v[176:179], a[128:131], v[36:39]
v_mfma_f32_16x16x32_bf16 v[60:63], v[180:183], a[132:135], v[60:63]
v_mfma_f32_16x16x32_bf16 v[40:43], v[180:183], a[128:131], v[40:43]
v_mfma_f32_16x16x32_bf16 v[64:67], v[184:187], a[132:135], v[64:67]
v_mfma_f32_16x16x32_bf16 v[44:47], v[184:187], a[128:131], v[44:47]
v_mfma_f32_16x16x32_bf16 v[68:71], v[188:191], a[132:135], v[68:71]
v_mfma_f32_16x16x32_bf16 v[48:51], v[188:191], a[128:131], v[48:51]
s_waitcnt vmcnt(6)
v_mfma_f32_16x16x32_bf16 v[52:55], v[192:195], a[140:143], v[52:55]
v_mfma_f32_16x16x32_bf16 v[32:35], v[192:195], a[136:139], v[32:35]
v_mfma_f32_16x16x32_bf16 v[56:59], v[196:199], a[140:143], v[56:59]
v_mfma_f32_16x16x32_bf16 v[36:39], v[196:199], a[136:139], v[36:39]
v_mfma_f32_16x16x32_bf16 v[60:63], v[200:203], a[140:143], v[60:63]
v_mfma_f32_16x16x32_bf16 v[40:43], v[200:203], a[136:139], v[40:43]
v_mfma_f32_16x16x32_bf16 v[64:67], v[204:207], a[140:143], v[64:67]
v_mfma_f32_16x16x32_bf16 v[44:47], v[204:207], a[136:139], v[44:47]
v_mfma_f32_16x16x32_bf16 v[68:71], v[208:211], a[140:143], v[68:71]
v_mfma_f32_16x16x32_bf16 v[48:51], v[208:211], a[136:139], v[48:51]
s_waitcnt vmcnt(4)
v_mfma_f32_16x16x32_bf16 v[52:55], v[212:215], a[148:151], v[52:55]
v_mfma_f32_16x16x32_bf16 v[32:35], v[212:215], a[144:147], v[32:35]
v_mfma_f32_16x16x32_bf16 v[56:59], a[0:3], a[148:151], v[56:59]
v_mfma_f32_16x16x32_bf16 v[36:39], a[0:3], a[144:147], v[36:39]
v_mfma_f32_16x16x32_bf16 v[60:63], a[4:7], a[148:151], v[60:63]
v_mfma_f32_16x16x32_bf16 v[40:43], a[4:7], a[144:147], v[40:43]
v_mfma_f32_16x16x32_bf16 v[64:67], a[8:11], a[148:151], v[64:67]
v_mfma_f32_16x16x32_bf16 v[44:47], a[8:11], a[144:147], v[44:47]
v_mfma_f32_16x16x32_bf16 v[68:71], a[12:15], a[148:151], v[68:71]
v_mfma_f32_16x16x32_bf16 v[48:51], a[12:15], a[144:147], v[48:51]
s_waitcnt vmcnt(2)
v_mfma_f32_16x16x32_bf16 v[52:55], a[16:19], a[156:159], v[52:55]
v_mfma_f32_16x16x32_bf16 v[32:35], a[16:19], a[152:155], v[32:35]
v_mfma_f32_16x16x32_bf16 v[56:59], a[20:23], a[156:159], v[56:59]
v_mfma_f32_16x16x32_bf16 v[36:39], a[20:23], a[152:155], v[36:39]
v_mfma_f32_16x16x32_bf16 v[60:63], a[24:27], a[156:159], v[60:63]
v_mfma_f32_16x16x32_bf16 v[40:43], a[24:27], a[152:155], v[40:43]
v_mfma_f32_16x16x32_bf16 v[64:67], a[28:31], a[156:159], v[64:67]
v_mfma_f32_16x16x32_bf16 v[44:47], a[28:31], a[152:155], v[44:47]
v_mfma_f32_16x16x32_bf16 v[68:71], a[32:35], a[156:159], v[68:71]
v_mfma_f32_16x16x32_bf16 v[48:51], a[32:35], a[152:155], v[48:51]
s_cmp_ge_u32 s47, 2
s_waitcnt vmcnt(0)
s_cbranch_scc1 .Lmk_f2skip_51
v_mfma_f32_16x16x32_bf16 v[52:55], a[36:39], v[116:119], v[52:55]
v_mfma_f32_16x16x32_bf16 v[32:35], a[36:39], v[112:115], v[32:35]
v_mfma_f32_16x16x32_bf16 v[56:59], a[40:43], v[116:119], v[56:59]
v_mfma_f32_16x16x32_bf16 v[36:39], a[40:43], v[112:115], v[36:39]
v_mfma_f32_16x16x32_bf16 v[60:63], a[44:47], v[116:119], v[60:63]
v_mfma_f32_16x16x32_bf16 v[40:43], a[44:47], v[112:115], v[40:43]
v_mfma_f32_16x16x32_bf16 v[64:67], a[48:51], v[116:119], v[64:67]
v_mfma_f32_16x16x32_bf16 v[44:47], a[48:51], v[112:115], v[44:47]
v_mfma_f32_16x16x32_bf16 v[68:71], a[52:55], v[116:119], v[68:71]
v_mfma_f32_16x16x32_bf16 v[48:51], a[52:55], v[112:115], v[48:51]
.Lmk_f2skip_51:
s_nop 4
v_xor_b32 v16, 1024, v123
ds_write_b128 v120, v[52:55] offset:0
ds_write_b128 v120, v[56:59] offset:1024
ds_write_b128 v122, v[60:63] offset:0
ds_write_b128 v122, v[64:67] offset:1024
ds_write_b128 v121, v[40:43] offset:0
ds_write_b128 v121, v[44:47] offset:1024
ds_write_b128 v16, v[68:71]
ds_write_b128 v123, v[48:51]
s_mul_i32 s50, s47, 6144
s_add_u32 s50, s50, 34816
v_add_u32 v16, s50, v2
s_and_b32 s51, s47, 1
s_lshl_b32 s51, s51, 10
s_lshl_b32 s52, s71, 9
s_add_u32 s51, s51, s52
s_add_u32 s51, s51, 59392
v_and_b32 v17, 496, v2
v_add_u32 v17, s51, v17
s_waitcnt lgkmcnt(0)
s_barrier
ds_read_b128 v[72:75], v16 offset:0
ds_read_b128 v[76:79], v16 offset:1024
ds_read_b128 v[80:83], v16 offset:2048
ds_read_b128 v[84:87], v16 offset:3072
ds_read_b128 v[88:91], v16 offset:4096
ds_read_b128 v[92:95], v16 offset:5120
ds_read_b128 v[96:99], v17 offset:0
ds_read_b128 v[100:103], v17 offset:2048
ds_read_b128 v[104:107], v17 offset:4096
ds_read_b128 v[108:111], v17 offset:6144
s_waitcnt lgkmcnt(8)
v_pk_add_f32 v[32:33], v[32:33], v[72:73]
v_pk_add_f32 v[34:35], v[34:35], v[74:75]
v_pk_add_f32 v[36:37], v[36:37], v[76:77]
v_pk_add_f32 v[38:39], v[38:39], v[78:79]
s_waitcnt lgkmcnt(6)
v_pk_add_f32 v[32:33], v[32:33], v[80:81]
v_pk_add_f32 v[34:35], v[34:35], v[82:83]
v_pk_add_f32 v[36:37], v[36:37], v[84:85]
v_pk_add_f32 v[38:39], v[38:39], v[86:87]
s_waitcnt lgkmcnt(4)
v_pk_add_f32 v[32:33], v[32:33], v[88:89]
v_pk_add_f32 v[34:35], v[34:35], v[90:91]
v_pk_add_f32 v[36:37], v[36:37], v[92:93]
v_pk_add_f32 v[38:39], v[38:39], v[94:95]
s_waitcnt lgkmcnt(2)
v_pk_add_f32 v[96:97], v[96:97], v[100:101]
v_pk_add_f32 v[98:99], v[98:99], v[102:103]
s_waitcnt lgkmcnt(0)
v_pk_add_f32 v[104:105], v[104:105], v[108:109]
v_pk_add_f32 v[106:107], v[106:107], v[110:111]
v_pk_add_f32 v[96:97], v[96:97], v[104:105]
v_pk_add_f32 v[98:99], v[98:99], v[106:107]
v_lshlrev_b32 v52, 16, v10
v_and_b32 v53, 0xffff0000, v10
v_pk_add_f32 v[32:33], v[32:33], v[52:53]
v_lshlrev_b32 v52, 16, v11
v_and_b32 v53, 0xffff0000, v11
v_pk_add_f32 v[34:35], v[34:35], v[52:53]
v_lshlrev_b32 v52, 16, v12
v_and_b32 v53, 0xffff0000, v12
v_pk_add_f32 v[36:37], v[36:37], v[52:53]
v_lshlrev_b32 v52, 16, v13
v_and_b32 v53, 0xffff0000, v13
v_pk_add_f32 v[38:39], v[38:39], v[52:53]
v_lshlrev_b32 v52, 16, v14
v_and_b32 v53, 0xffff0000, v14
v_pk_add_f32 v[96:97], v[96:97], v[52:53]
v_lshlrev_b32 v52, 16, v15
v_and_b32 v53, 0xffff0000, v15
v_pk_add_f32 v[98:99], v[98:99], v[52:53]
v_lshlrev_b32 v52, 16, v26
v_and_b32 v53, 0xffff0000, v26
v_pk_add_f32 v[32:33], v[32:33], v[52:53]
v_lshlrev_b32 v52, 16, v27
v_and_b32 v53, 0xffff0000, v27
v_pk_add_f32 v[34:35], v[34:35], v[52:53]
v_lshlrev_b32 v52, 16, v28
v_and_b32 v53, 0xffff0000, v28
v_pk_add_f32 v[36:37], v[36:37], v[52:53]
v_lshlrev_b32 v52, 16, v29
v_and_b32 v53, 0xffff0000, v29
v_pk_add_f32 v[38:39], v[38:39], v[52:53]
v_lshlrev_b32 v52, 16, v30
v_and_b32 v53, 0xffff0000, v30
v_pk_add_f32 v[96:97], v[96:97], v[52:53]
v_lshlrev_b32 v52, 16, v31
v_and_b32 v53, 0xffff0000, v31
v_pk_add_f32 v[98:99], v[98:99], v[52:53]
v_cvt_pk_bf16_f32 v56, v32, v33
v_cvt_pk_bf16_f32 v57, v34, v35
v_cvt_pk_bf16_f32 v58, v36, v37
v_cvt_pk_bf16_f32 v59, v38, v39
v_cvt_pk_bf16_f32 v60, v96, v97
v_cvt_pk_bf16_f32 v61, v98, v99
s_mov_b32 s54, 163840
buffer_store_dwordx4 v[56:59], v20, s[16:19], s54 offen
buffer_store_dwordx2 v[60:61], v21, s[16:19], s54 offen
s_or_b32 s50, s2, s47
s_cmp_lg_u32 s50, 0
s_cbranch_scc1 .Lmk_fin_52
v_mov_b32 v18, s44
v_mov_b32 v19, s45
s_mov_b64 exec, 0xffff
buffer_store_dwordx2 v[18:19], v3, s[12:15], 0 offen sc1
s_mov_b64 exec, -1
.Lmk_fin_52:
s_waitcnt vmcnt(0)
.Lmk_end:
)ASM"
      :
      : "{s[0:1]}"(__builtin_amdgcn_kernarg_segment_ptr()), "{s2}"(blockIdx.x), "{v0}"(threadIdx.x),
        "s"((unsigned)(size_t)(__attribute__((address_space(3))) char*)lds)
      : "memory", "v255", "a255", "s99", "vcc");
}

extern "C" void kernel_launch(void* const* d_in, const int* in_sizes, int n_in, void* d_out, int out_size, void* d_ws, size_t ws_size,
                              hipStream_t stream) {
  if (ws_size < 31457280) return;
  mk_block<<<256, 256, 0, stream>>>(d_in[0], d_in[1], d_in[2], d_in[4], d_in[5], d_in[6], d_in[7], d_out, d_ws);
}
```
